```python
import torch

def _softmax(x, axis=-1):
    return torch.softmax(x, dim=axis)
def _gelu(x):
    return torch.nn.functional.gelu(x, approximate="tanh")
def _silu(x):
    return torch.nn.functional.silu(x)
def _sigmoid(x):
    return torch.sigmoid(x)
def _layernorm(x, axis=-1, eps=1e-5):
    m = x.mean(dim=axis, keepdim=True)
    v = x.var(dim=axis, keepdim=True, unbiased=False)
    return (x - m) / torch.sqrt(v + eps)
def _scatter_add(tgt, idx, src, axis=0):
    return tgt.clone().index_add_(axis, idx, src)
def _segment_sum(src, seg, n):
    out = torch.zeros((n,) + src.shape[1:], dtype=src.dtype)
    return out.index_add_(0, seg, src)
def _pad(x, pw):
    flat = tuple(v for pair in reversed(pw) for v in pair)
    return torch.nn.functional.pad(x, flat)

kernel_name = 'concat_heads__grouped_kv_mqa__compressed_kv_cla__prompt'

def setup_inputs():

    _w = (lambda a: (lambda t: t.to(torch.bfloat16) if t.is_floating_point() else t)(torch.as_tensor(a)))
    def _A_setup():
        B, S, D, H = 1, 2048, 1024, 32
        Dh = D // H
        return {
            "x": _w((lambda *s: torch.randn(*s, dtype=torch.bfloat16) * 0.05)(B, S, D)),
            "wq": _w((lambda *s: torch.randn(*s, dtype=torch.bfloat16) * 0.8)(D, H * Dh)),
            "wk": _w((lambda *s: torch.randn(*s, dtype=torch.bfloat16) * 0.8)(D, Dh)),
            "wv": _w((lambda *s: torch.randn(*s, dtype=torch.bfloat16) * 0.05)(D, Dh)),
            "wo": _w((lambda *s: torch.randn(*s, dtype=torch.bfloat16) * 0.05)(H * Dh, D)),
        }
    _A_in = _A_setup()

    def _B_setup():
        B, H, Sq, Sk, D, M = 1, 32, 2048, 2048, 128, 1024
        Dc = 4 * D
        return {
            "x": _w((lambda *s: torch.randn(*s, dtype=torch.bfloat16) * 1.6)(B, Sq, M)),
            "c_kv": _w((lambda *s: torch.randn(*s, dtype=torch.bfloat16) * 0.2)(B, Sk, Dc)),
            "w_q": _w((lambda *s: torch.randn(*s, dtype=torch.bfloat16) * 6.4)(M, H, D) * (M ** -0.5)),
            "w_uk": _w((lambda *s: torch.randn(*s, dtype=torch.bfloat16) * 6.4)(Dc, H, D) * (Dc ** -0.5)),
            "w_uv": _w((lambda *s: torch.randn(*s, dtype=torch.bfloat16) * 0.2)(Dc, H, D) * (Dc ** -0.5)),
            "w_o": _w((lambda *s: torch.randn(*s, dtype=torch.bfloat16) * 0.2)(H, D, M) * ((H * D) ** -0.5)),
        }
    _B_in = _B_setup()

    return {
        "A_x": _A_in["x"],
        "A_wq": _A_in["wq"],
        "A_wk": _A_in["wk"],
        "A_wv": _A_in["wv"],
        "A_wo": _A_in["wo"],
        "B_x": _B_in["x"],
        "B_c_kv": _B_in["c_kv"],
        "B_w_q": _B_in["w_q"],
        "B_w_uk": _B_in["w_uk"],
        "B_w_uv": _B_in["w_uv"],
        "B_w_o": _B_in["w_o"],
    }

def reference(A_x, A_wq, A_wk, A_wv, A_wo, B_x, B_c_kv, B_w_q, B_w_uk, B_w_uv, B_w_o):
    def _A_ref(x, wq, wk, wv, wo):
        B, S, D = x.shape
        H = 32
        Dh = D // H
        q = (lambda x, *d: x.permute(*d))((x @ wq).reshape(B, S, H, Dh), 0, 2, 1, 3)
        k = x @ wk
        v = x @ wv
        scores = torch.einsum("bhqd,bkd->bhqk", q, k) * (Dh ** -0.5)
        p = _softmax(scores, axis=-1)
        o = torch.einsum("bhqk,bkd->bhqd", p, v)
        return (lambda x, *d: x.permute(*d))(o, 0, 2, 1, 3).reshape(B, S, H * Dh) @ wo

    def _B_ref(x, c_kv, w_q, w_uk, w_uv, w_o):
        B, Sq, M = x.shape[0], x.shape[1], x.shape[2]
        Sk = c_kv.shape[1]
        H, D = w_q.shape[1], w_q.shape[2]
        q = torch.einsum("bqm,mhd->bhqd", x, w_q)
        k = torch.einsum("bkc,chd->bhkd", c_kv, w_uk)
        v = torch.einsum("bkc,chd->bhkd", c_kv, w_uv)
        scores = torch.einsum("bhqd,bhkd->bhqk", q, k) * (D ** -0.5)
        off = Sk - Sq
        qi = torch.arange(Sq)[:, None]
        kj = torch.arange(Sk)[None, :]
        cmask = kj <= (qi + off)
        scores = torch.where(cmask[None, None], scores, -1e9)
        p = _softmax(scores, axis=-1)
        ctx = torch.einsum("bhqk,bhkd->bhqd", p, v)
        return torch.einsum("bhqd,hdm->bqm", ctx, w_o)

    a = _A_ref(A_x, A_wq, A_wk, A_wv, A_wo)
    b = _B_ref(B_x, B_c_kv, B_w_q, B_w_uk, B_w_uv, B_w_o)
    return torch.concatenate([a, b], axis=-1)
```

```cpp
#include <hip/hip_runtime.h>
#include <hip/hip_bf16.h>
#include <stdint.h>

typedef unsigned short u16;
__device__ __forceinline__ float bf2f(u16 b) { return __uint_as_float(((unsigned)b) << 16); }
__device__ __forceinline__ u16 f2bf(float f) { __hip_bfloat16 h = __float2bfloat16(f); return *reinterpret_cast<u16*>(&h); }

__global__ void gemm_naive(const u16* A, int lda, const u16* B, int ldb, u16* C, int ldc, int M, int N, int K) {
    __shared__ float As[16][17], Bs[16][17];
    int tx = threadIdx.x, ty = threadIdx.y;
    int row = blockIdx.y * 16 + ty, col = blockIdx.x * 16 + tx;
    float acc = 0.f;
    for (int k0 = 0; k0 < K; k0 += 16) {
        As[ty][tx] = bf2f(A[(size_t)row * lda + k0 + tx]);
        Bs[ty][tx] = bf2f(B[(size_t)(k0 + ty) * ldb + col]);
        __syncthreads();
        for (int k = 0; k < 16; ++k) acc += As[ty][k] * Bs[k][tx];
        __syncthreads();
    }
    C[(size_t)row * ldc + col] = f2bf(acc);
}

template <int D, bool CAUSAL>
__global__ void attn_naive(const u16* Q, int ldq, const u16* K, const u16* V, int ldk, int kvhs, u16* O, int ldo, int S, float scale) {
    __shared__ float sc[2048];
    __shared__ float qs[D];
    __shared__ float red[256];
    int h = blockIdx.y, qi = blockIdx.x, t = threadIdx.x;
    if (t < D) qs[t] = bf2f(Q[(size_t)qi * ldq + h * D + t]);
    __syncthreads();
    int nk = CAUSAL ? qi + 1 : S;
    float m = -1e30f;
    for (int j = t; j < nk; j += 256) {
        float s = 0.f;
        for (int d = 0; d < D; ++d) s += qs[d] * bf2f(K[(size_t)j * ldk + h * kvhs + d]);
        s *= scale; sc[j] = s; m = fmaxf(m, s);
    }
    red[t] = m; __syncthreads();
    for (int o = 128; o > 0; o >>= 1) { if (t < o) red[t] = fmaxf(red[t], red[t + o]); __syncthreads(); }
    m = red[0]; __syncthreads();
    float l = 0.f;
    for (int j = t; j < nk; j += 256) { float p = __expf(sc[j] - m); sc[j] = p; l += p; }
    red[t] = l; __syncthreads();
    for (int o = 128; o > 0; o >>= 1) { if (t < o) red[t] += red[t + o]; __syncthreads(); }
    l = red[0]; __syncthreads();
    if (t < D) {
        float acc = 0.f;
        for (int j = 0; j < nk; ++j) acc += sc[j] * bf2f(V[(size_t)j * ldk + h * kvhs + t]);
        O[(size_t)qi * ldo + h * D + t] = f2bf(acc / l);
    }
}

extern "C" void kernel_launch(void* const* d_in, const int* in_sizes, int n_in,
                              void* d_out, int out_size, void* d_ws, size_t ws_size,
                              hipStream_t stream) {
    const u16* A_x = (const u16*)d_in[0];
    const u16* A_wq = (const u16*)d_in[1];
    const u16* A_wk = (const u16*)d_in[2];
    const u16* A_wv = (const u16*)d_in[3];
    const u16* A_wo = (const u16*)d_in[4];
    const u16* B_x = (const u16*)d_in[5];
    const u16* B_ckv = (const u16*)d_in[6];
    const u16* B_wq = (const u16*)d_in[7];
    const u16* B_wuk = (const u16*)d_in[8];
    const u16* B_wuv = (const u16*)d_in[9];
    const u16* B_wo = (const u16*)d_in[10];
    u16* out = (u16*)d_out;
    u16* ws = (u16*)d_ws;
    const int S = 2048;
    u16* qA = ws;
    u16* kA = qA + S * 1024;
    u16* vA = kA + S * 32;
    u16* oA = vA + S * 32;
    u16* qB = oA + S * 1024;
    u16* kB = qB + S * 4096;
    u16* vB = kB + S * 4096;
    u16* oB = vB + S * 4096;
    dim3 blk(16, 16);
    gemm_naive<<<dim3(1024 / 16, S / 16), blk, 0, stream>>>(A_x, 1024, A_wq, 1024, qA, 1024, S, 1024, 1024);
    gemm_naive<<<dim3(32 / 16, S / 16), blk, 0, stream>>>(A_x, 1024, A_wk, 32, kA, 32, S, 32, 1024);
    gemm_naive<<<dim3(32 / 16, S / 16), blk, 0, stream>>>(A_x, 1024, A_wv, 32, vA, 32, S, 32, 1024);
    gemm_naive<<<dim3(4096 / 16, S / 16), blk, 0, stream>>>(B_x, 1024, B_wq, 4096, qB, 4096, S, 4096, 1024);
    gemm_naive<<<dim3(4096 / 16, S / 16), blk, 0, stream>>>(B_ckv, 512, B_wuk, 4096, kB, 4096, S, 4096, 512);
    gemm_naive<<<dim3(4096 / 16, S / 16), blk, 0, stream>>>(B_ckv, 512, B_wuv, 4096, vB, 4096, S, 4096, 512);
    attn_naive<32, false><<<dim3(S, 32), 256, 0, stream>>>(qA, 1024, kA, vA, 32, 0, oA, 1024, S, 0.17677669529663687f);
    attn_naive<128, true><<<dim3(S, 32), 256, 0, stream>>>(qB, 4096, kB, vB, 4096, 128, oB, 4096, S, 0.08838834764831845f);
    gemm_naive<<<dim3(1024 / 16, S / 16), blk, 0, stream>>>(oA, 1024, A_wo, 1024, out, 2048, S, 1024, 1024);
    gemm_naive<<<dim3(1024 / 16, S / 16), blk, 0, stream>>>(oB, 4096, B_wo, 1024, out + 1024, 2048, S, 1024, 4096);
}
```

```cpp
#include <hip/hip_runtime.h>
#include <stdint.h>

#define LAS __attribute__((address_space(3)))
#define DI __device__ __forceinline__
typedef unsigned short u16;
typedef short bf16x8 __attribute__((ext_vector_type(8)));
typedef short s16x4 __attribute__((ext_vector_type(4)));
typedef float f32x4 __attribute__((ext_vector_type(4)));
typedef float f32x16 __attribute__((ext_vector_type(16)));
typedef unsigned u32x4 __attribute__((ext_vector_type(4)));
typedef unsigned u32x2 __attribute__((ext_vector_type(2)));
typedef LAS unsigned char* ldsp;

constexpr size_t WS_QB = 0;
constexpr size_t WS_KB = WS_QB + (16u << 20);
constexpr size_t WS_VB = WS_KB + (16u << 20);
constexpr size_t WS_QA = WS_VB + (16u << 20);
constexpr size_t WS_KA = WS_QA + (4u << 20);
constexpr size_t WS_VA = WS_KA + (128u << 10);
constexpr size_t WS_CTX = WS_VA + (128u << 10);
constexpr size_t WS_OA = WS_CTX + (16u << 20);
constexpr size_t WS_PART = WS_OA + (4u << 20);
constexpr size_t WS_FLAG = WS_PART + (8u << 20);

constexpr float LOG2E = 1.4426950408889634f;
constexpr float SCALE_A = 0.17677669529663687f * LOG2E;
constexpr float SCALE_B = 0.08838834764831845f * LOG2E;
constexpr float THR = 40.f;

#define WAIT_V(n) asm volatile("s_waitcnt vmcnt(" #n ")" ::: "memory")
#define WAIT_L(n) asm volatile("s_waitcnt lgkmcnt(" #n ")" ::: "memory")
#define BAR __builtin_amdgcn_s_barrier()
#define SCHED __builtin_amdgcn_sched_barrier(0)
#define MFMA16(a, b, c) __builtin_amdgcn_mfma_f32_16x16x32_bf16(a, b, c, 0, 0, 0)
#define MFMA32(a, b, c) __builtin_amdgcn_mfma_f32_32x32x16_bf16(a, b, c, 0, 0, 0)

typedef float f32x2 __attribute__((ext_vector_type(2)));
typedef __bf16 bf16x2_t __attribute__((ext_vector_type(2)));
DI unsigned cvtpk(float lo, float hi) { const f32x2 v = {lo, hi}; return __builtin_bit_cast(unsigned, __builtin_convertvector(v, bf16x2_t)); }
DI void glds16(const char* sbase, unsigned voff, unsigned ldsaddr) {
    unsigned keep;
    asm volatile("s_mov_b32 %0, m0\n\ts_mov_b32 m0, %3\n\ts_nop 0\n\tglobal_load_lds_dwordx4 %1, %2\n\ts_mov_b32 m0, %0"
                 : "=&s"(keep) : "v"(voff), "s"(sbase), "s"(ldsaddr) : "memory");
}
DI unsigned ldsaddr_of(ldsp p) { return (unsigned)(size_t)p; }
DI bf16x8 ldsr(ldsp p) { return *(const LAS bf16x8*)p; }
DI s16x4 ldstr(ldsp p) { return __builtin_amdgcn_ds_read_tr16_b64_v4i16((LAS s16x4*)p); }
DI bf16x8 ldstr2(ldsp p) { const s16x4 a = ldstr(p), b = ldstr(p + 256); return (bf16x8){a[0], a[1], a[2], a[3], b[0], b[1], b[2], b[3]}; }
DI int rfl(int x) { return __builtin_amdgcn_readfirstlane(x); }
#define STAMP(ws, kern, i) do {} while (0)
#define TSDECL do {} while (0)
#define TS(i) do {} while (0)
#define TSOUT(ws, slot) do {} while (0)
#define KEEP(...) do {} while (0)

DI void warm(const char* base, int npieces, unsigned dump) {
    const int wid = rfl(threadIdx.x >> 6); const unsigned l16 = (threadIdx.x & 63) * 16;
    for (int i = 0; i < npieces; ++i) glds16(base + (size_t)i * 8192 + wid * 1024, l16, dump + wid * 1024);
}

constexpr int HTB = 16384;
#define SA(b, h) (((b) * 2 + (h)) * HTB)
#define SB(b, h) ((4 + (b) * 2 + (h)) * HTB)

template <bool SWAP>
DI void gemm_big(ldsp lds, const char* gA, unsigned lda2, const char* gW, unsigned ldw2, int nt, int nunits,
                 char* out, int row0, int head0, float scale) {
    const int tid = threadIdx.x, wid = rfl(tid >> 6), lane = tid & 63, wr = wid >> 2, wc = wid & 3, fr = lane & 15, fq = lane >> 4;
    unsigned voffA[2], voffW[2];
#pragma unroll
    for (int i = 0; i < 2; ++i) {
        voffA[i] = (unsigned)((tid >> 3) + 64 * i) * lda2 + (unsigned)(((tid & 7) ^ ((tid >> 3) & 7)) << 4);
        const int kg = (tid >> 7) + 4 * i, ng = (tid >> 5) & 3, r = (tid >> 2) & 7, c = (tid & 3) ^ (((tid >> 7) & 1) << 1);
        voffW[i] = (unsigned)(kg * 8 + r) * ldw2 + (unsigned)(ng * 64 + c * 16);
    }
    const size_t kstepA = 128, kstepW = (size_t)64 * ldw2, hstepA = (size_t)128 * lda2, hstepW = 256;
    const unsigned ldsw = ldsaddr_of(lds) + (unsigned)wid * 1024u;
    const int rowb = wr * 64 + (SWAP ? 16 * (fr >> 3) + (fr & 7) : fr);
    const int aoff0 = rowb * 128 + ((fq ^ (fr & 7)) << 4), aoff1 = aoff0 ^ 64;
    const int boff0 = (fq * 4 + wc) * 512 + ((lane >> 2) & 3) * 64 + (((lane & 3) * 8) ^ ((fq & 1) << 5)), boff1 = boff0 ^ 32;
#define MOFF(m) (SWAP ? ((m) >> 1) * 4096 + ((m) & 1) * 1024 : (m) * 2048)
#define STAGE(bufoff, gbase, voff) do { _Pragma("unroll") for (int _i = 0; _i < 2; ++_i) glds16((gbase), (voff)[_i], (bufoff) + ldsw + _i * 8192); } while (0)
#define LDA(dst, b, h) do { _Pragma("unroll") for (int m = 0; m < 4; ++m) { dst[m][0] = ldsr(lds + SA(b, h) + aoff0 + MOFF(m)); dst[m][1] = ldsr(lds + SA(b, h) + aoff1 + MOFF(m)); } } while (0)
#define LDB(dst, b, h) do { _Pragma("unroll") for (int k = 0; k < 2; ++k) { dst[0][k] = ldstr2(lds + SB(b, h) + boff0 + k * 8192); dst[1][k] = ldstr2(lds + SB(b, h) + boff1 + k * 8192); } } while (0)
#define MMA(ai, bj, At, Bt) do { __builtin_amdgcn_s_setprio(1); _Pragma("unroll") for (int m = 0; m < 4; ++m) _Pragma("unroll") for (int n = 0; n < 2; ++n) _Pragma("unroll") for (int k = 0; k < 2; ++k) \
        acc[ai][bj][m][n] = SWAP ? MFMA16(At[m][k], Bt[n][k], acc[ai][bj][m][n]) : MFMA16(Bt[n][k], At[m][k], acc[ai][bj][m][n]); __builtin_amdgcn_s_setprio(0); } while (0)
    f32x4 acc[2][2][4][2];
#pragma unroll
    for (int a = 0; a < 2; ++a)
#pragma unroll
        for (int b = 0; b < 2; ++b)
#pragma unroll
            for (int m = 0; m < 4; ++m)
#pragma unroll
                for (int n = 0; n < 2; ++n) acc[a][b][m][n] = (f32x4){0.f, 0.f, 0.f, 0.f};
    bf16x8 At[4][2], B0[2][2], B1[2][2];
    const char* cA = gA; const char* cW = gW;
    STAGE(SB(0, 0), cW, voffW); STAGE(SA(0, 0), cA, voffA); STAGE(SB(0, 1), cW + hstepW, voffW); STAGE(SA(0, 1), cA + hstepA, voffA);
    if (wr == 1) BAR;
    WAIT_V(4); BAR;
    STAGE(SB(1, 0), cW + kstepW, voffW); STAGE(SA(1, 0), cA + kstepA, voffA); STAGE(SB(1, 1), cW + hstepW + kstepW, voffW);
    WAIT_V(6); BAR;
    for (int ui = 0;; ++ui) {
        const bool has_next = ui + 1 < nunits;
        const char* nW = has_next ? cW + 512 : cW;
        for (int t = 0; t < nt; t += 2) {
            const bool last = (t == nt - 2);
            const char* a1 = cA + (size_t)(t + 1) * kstepA;
            const char* a2 = last ? cA : cA + (size_t)(t + 2) * kstepA; const char* b2 = last ? nW : cW + (size_t)(t + 2) * kstepW;
            const char* a3 = a2 + kstepA; const char* b3 = b2 + kstepW;
            LDB(B0, 0, 0); SCHED; LDA(At, 0, 0); STAGE(SA(1, 1), a1 + hstepA, voffA);
            WAIT_L(8); BAR; WAIT_L(0); MMA(0, 0, At, B0); BAR; SCHED;
            LDB(B1, 0, 1); STAGE(SB(0, 0), b2, voffW);
            BAR; WAIT_L(0); MMA(0, 1, At, B1); BAR;
            LDA(At, 0, 1); STAGE(SA(0, 0), a2, voffA);
            BAR; WAIT_L(0); MMA(1, 0, At, B0); BAR; SCHED;
            STAGE(SB(0, 1), b2 + hstepW, voffW);
            WAIT_V(6); BAR; MMA(1, 1, At, B1); BAR;
            LDB(B0, 1, 0); SCHED; LDA(At, 1, 0); STAGE(SA(0, 1), a2 + hstepA, voffA);
            WAIT_L(8); BAR; WAIT_L(0); MMA(0, 0, At, B0); BAR; SCHED;
            LDB(B1, 1, 1); STAGE(SB(1, 0), b3, voffW);
            BAR; WAIT_L(0); MMA(0, 1, At, B1); BAR;
            LDA(At, 1, 1); STAGE(SA(1, 0), a3, voffA);
            BAR; WAIT_L(0); MMA(1, 0, At, B0); BAR; SCHED;
            STAGE(SB(1, 1), b3 + hstepW, voffW);
            WAIT_V(6); BAR; MMA(1, 1, At, B1); BAR;
        }
        const int h0 = head0 + 2 * ui;
        if constexpr (!SWAP) {
#pragma unroll
            for (int ai = 0; ai < 2; ++ai)
#pragma unroll
                for (int m = 0; m < 4; ++m) {
                    const int t = row0 + ai * 128 + wr * 64 + m * 16 + fr;
#pragma unroll
                    for (int bj = 0; bj < 2; ++bj) {
                        const f32x4 v0 = acc[ai][bj][m][0] * scale, v1 = acc[ai][bj][m][1] * scale;
                        const auto r0 = __builtin_amdgcn_permlane16_swap(cvtpk(v0[0], v0[1]), cvtpk(v1[0], v1[1]), false, false);
                        const auto r1 = __builtin_amdgcn_permlane16_swap(cvtpk(v0[2], v0[3]), cvtpk(v1[2], v1[3]), false, false);
                        const u32x4 w = {r0[0], r1[0], r0[1], r1[1]};
                        const size_t off = ((size_t)(((h0 + bj) * 64 + (t >> 5)) * 8 + 2 * wc + (fq & 1))) * 1024 + (size_t)(((t & 31) + 32 * (fq >> 1)) * 16);
                        *(u32x4*)(out + off) = w;
                    }
                }
        } else {
#pragma unroll
            for (int ai = 0; ai < 2; ++ai)
#pragma unroll
                for (int mp = 0; mp < 2; ++mp) {
                    const int pos = row0 + ai * 128 + wr * 64 + 32 * mp + 8 * fq;
#pragma unroll
                    for (int bj = 0; bj < 2; ++bj)
#pragma unroll
                        for (int n = 0; n < 2; ++n) {
                            const f32x4 a = acc[ai][bj][2 * mp][n], b = acc[ai][bj][2 * mp + 1][n];
                            const u32x4 w = {cvtpk(a[0], a[1]), cvtpk(a[2], a[3]), cvtpk(b[0], b[1]), cvtpk(b[2], b[3])};
                            const size_t off = ((size_t)(((h0 + bj) * 128 + (pos >> 4)) * 4 + wc)) * 1024 + (size_t)((n * 16 + fr + 32 * (fq & 1)) * 16);
                            *(u32x4*)(out + off) = w;
                        }
                }
        }
        if (!has_next) break;
#pragma unroll
        for (int a = 0; a < 2; ++a)
#pragma unroll
            for (int b = 0; b < 2; ++b)
#pragma unroll
                for (int m = 0; m < 4; ++m)
#pragma unroll
                    for (int n = 0; n < 2; ++n) acc[a][b][m][n] = (f32x4){0.f, 0.f, 0.f, 0.f};
        cW = nW;
    }
    WAIT_V(16);
    if (wr == 0) BAR;
    BAR;
#undef MOFF
#undef STAGE
#undef LDA
#undef LDB
#undef MMA
}

constexpr int SG_STAGE = 28672;
DI void gemm_small(ldsp lds, const char* xA, const char* wq, const char* wx  , int rt, int ct, char* ws) {
    const int tid = threadIdx.x, wid = rfl(tid >> 6), lane = tid & 63, wr = wid >> 1, wc = wid & 1, fr = lane & 15, fq = lane >> 4;
    const bool extra = ct < 2;
    const char* gA = xA + (size_t)rt * 128 * 2048;
    const char* gW = wq + (size_t)ct * 128;
    unsigned voffA[2];
#pragma unroll
    for (int i = 0; i < 2; ++i) voffA[i] = (unsigned)((tid >> 3) + 64 * i) * 2048u + (unsigned)(((tid & 7) ^ ((tid >> 3) & 7)) << 4);
    const int cch = (lane & 3) ^ ((wid & 1) << 1);
    const unsigned voffW = (unsigned)(wid * 8 + ((lane >> 2) & 7)) * 2048u + (unsigned)((lane >> 5) * 64 + cch * 16);
    const unsigned voffX = (unsigned)(wid * 8 + ((lane >> 2) & 7)) * 64u + (unsigned)(cch * 16);
    const int aoff0 = (wr * 32 + fr) * 128 + ((fq ^ (fr & 7)) << 4), aoff1 = aoff0 ^ 64;
    const int boff0 = 16384 + (fq * 2 + wc) * 512 + ((lane >> 2) & 3) * 64 + (((lane & 3) * 8) ^ ((fq & 1) << 5)), boff1 = boff0 ^ 32;
    const int xoff = 24576 + fq * 512 + ((lane >> 2) & 3) * 64 + (((wc * 32) + (lane & 3) * 8) ^ ((fq & 1) << 5));
#define SPIECE(t, i) do { const int _t = (t) < 16 ? (t) : 15; const unsigned _b = ldsaddr_of(lds) + ((t) & 3) * SG_STAGE; \
        if ((i) < 2) glds16(gA + _t * 128, voffA[(i) & 1], _b + wid * 1024 + ((i) & 1) * 8192); \
        else if ((i) == 2) glds16(gW + (size_t)_t * 64 * 2048, voffW, _b + 16384 + wid * 1024); \
        else if (extra) { const unsigned _x = (unsigned)rfl((int)(_b + 24576 + wid * 512)); if (lane < 32) glds16(wx + _t * 64 * 64, voffX, _x); } } while (0)
#define SFENCE do { asm volatile("" ::: "memory"); __builtin_amdgcn_sched_barrier(0); } while (0)
#define SRD(AF, BF, XF, t) do { ldsp _b = lds + ((t) & 3) * SG_STAGE; \
        _Pragma("unroll") for (int k = 0; k < 2; ++k) { BF[0][k] = ldstr2(_b + boff0 + k * 4096); BF[1][k] = ldstr2(_b + boff1 + k * 4096); } \
        _Pragma("unroll") for (int m = 0; m < 2; ++m) { AF[m][0] = ldsr(_b + aoff0 + m * 2048); AF[m][1] = ldsr(_b + aoff1 + m * 2048); } \
        if (extra) { XF[0] = ldstr2(_b + xoff); XF[1] = ldstr2(_b + xoff + 2048); } } while (0)
#define SMM(AF, BF, k) do { _Pragma("unroll") for (int m = 0; m < 2; ++m) _Pragma("unroll") for (int n = 0; n < 2; ++n) acc[m][n] = MFMA16(BF[n][k], AF[m][k], acc[m][n]); } while (0)
#define SSTEP(AX, BX, XX, AY, BY, XY, t) do { \
        if (extra) asm volatile("s_waitcnt vmcnt(4) lgkmcnt(0)\n\ts_barrier" ::: "memory"); else asm volatile("s_waitcnt vmcnt(3) lgkmcnt(0)\n\ts_barrier" ::: "memory"); SFENCE; \
        SRD(AY, BY, XY, (t) + 1); SFENCE; \
        SPIECE((t) + 3, 0); SMM(AX, BX, 0); SFENCE; \
        SPIECE((t) + 3, 1); SMM(AX, BX, 1); SFENCE; \
        SPIECE((t) + 3, 2); SPIECE((t) + 3, 3); \
        if (extra) { _Pragma("unroll") for (int k = 0; k < 2; ++k) _Pragma("unroll") for (int m = 0; m < 2; ++m) acc[m][2] = MFMA16(XX[k], AX[m][k], acc[m][2]); } SFENCE; \
    } while (0)
    f32x4 acc[2][3];
#pragma unroll
    for (int m = 0; m < 2; ++m)
#pragma unroll
        for (int n = 0; n < 3; ++n) acc[m][n] = (f32x4){0.f, 0.f, 0.f, 0.f};
    bf16x8 A0[2][2], B0[2][2], X0[2], A1[2][2], B1[2][2], X1[2];
    X0[0] = X0[1] = X1[0] = X1[1] = (bf16x8){0, 0, 0, 0, 0, 0, 0, 0};
#pragma unroll
    for (int i = 0; i < 4; ++i) SPIECE(0, i);
#pragma unroll
    for (int i = 0; i < 4; ++i) SPIECE(1, i);
#pragma unroll
    for (int i = 0; i < 4; ++i) SPIECE(2, i);
    if (extra) WAIT_V(8); else WAIT_V(6);
    BAR;
    SRD(A0, B0, X0, 0);
    for (int t = 0; t < 16; t += 2) {
        SSTEP(A0, B0, X0, A1, B1, X1, t);
        SSTEP(A1, B1, X1, A0, B0, X0, t + 1);
    }
#undef SPIECE
#undef SFENCE
#undef SRD
#undef SMM
#undef SSTEP
#pragma unroll
    for (int m = 0; m < 2; ++m) {
        const int t = rt * 128 + wr * 32 + m * 16 + fr;
        const int h = ct * 2 + wc;
#pragma unroll
        for (int n = 0; n < 2; ++n) {
            const f32x4 v = acc[m][n] * SCALE_A;
            const u32x2 w = {cvtpk(v[0], v[1]), cvtpk(v[2], v[3])};
            const size_t off = ((size_t)((h * 64 + (t >> 5)) * 2 + n)) * 1024 + (size_t)(((t & 31) + 32 * (fq >> 1)) * 16 + (fq & 1) * 8);
            *(u32x2*)(ws + WS_QA + off) = w;
        }
        if (ct == 0) {
            const f32x4 v = acc[m][2];
            const u32x2 w = {cvtpk(v[0], v[1]), cvtpk(v[2], v[3])};
            const size_t off = ((size_t)((t >> 5) * 2 + wc)) * 1024 + (size_t)(((t & 31) + 32 * (fq >> 1)) * 16 + (fq & 1) * 8);
            *(u32x2*)(ws + WS_KA + off) = w;
        } else if (ct == 1) {
            const f32x4 v = acc[m][2];
            const int pos = (t & ~12) | ((t & 4) << 1) | ((t & 8) >> 1);
            const unsigned w0 = cvtpk(v[0], v[1]), w1 = cvtpk(v[2], v[3]);
            const int d = wc * 16 + 4 * fq;
            char* p = ws + WS_VA + (size_t)(pos >> 4) * 1024 + (size_t)((d + 32 * ((pos >> 3) & 1)) * 16 + (pos & 7) * 2);
            *(u16*)(p) = (u16)w0; *(u16*)(p + 16) = (u16)(w0 >> 16); *(u16*)(p + 32) = (u16)w1; *(u16*)(p + 48) = (u16)(w1 >> 16);
        }
    }
    WAIT_V(0);
}

__global__ __launch_bounds__(512, 2) void k_proj(const char* __restrict__ xA, const char* __restrict__ wqA, const char* __restrict__ wkA, const char* __restrict__ wvA,
                                                 const char* __restrict__ xB, const char* __restrict__ ckv, const char* __restrict__ wqB,
                                                 const char* __restrict__ wuk, const char* __restrict__ wuv, char* __restrict__ ws) {
    extern __shared__ __attribute__((aligned(16))) unsigned char smem[];
    ldsp lds = (ldsp)smem;
    const int b = blockIdx.x, xcd = b & 7, idx = b >> 3;
    if (b == 0 && threadIdx.x < 128) *(unsigned*)(ws + WS_FLAG + threadIdx.x * 128) = 0u;
    STAMP(ws, 0, 0);
    warm(xA + (size_t)b * 16384, 2, ldsaddr_of(lds) + 131072);
    warm(wqA + (size_t)b * 8192, 1, ldsaddr_of(lds) + 131072);
    if (idx < 16) {
        const int rt = 4 * (xcd & 1) + (idx & 3), ct = 4 * (xcd >> 1) + (idx >> 2);
        gemm_big<false>(lds, xB + (size_t)rt * 256 * 2048, 2048u, wqB + (size_t)ct * 512, 8192u, 16, 1, ws + WS_QB, rt * 256, ct * 2, SCALE_B);
    } else if (idx < 24) {
        const int i = idx - 16, rt = 4 * (xcd & 1) + (i & 3), cp = 2 * (xcd >> 1) + (i >> 2);
        gemm_big<false>(lds, ckv + (size_t)rt * 256 * 1024, 1024u, wuk + (size_t)cp * 1024, 8192u, 8, 2, ws + WS_KB, rt * 256, cp * 4, 1.f);
    } else {
        const int i = idx - 24, rt = 4 * (xcd & 1) + (i & 3), cp = 2 * (xcd >> 1) + (i >> 2);
        gemm_big<true>(lds, ckv + (size_t)rt * 256 * 1024, 1024u, wuv + (size_t)cp * 1024, 8192u, 8, 2, ws + WS_VB, rt * 256, cp * 4, 1.f);
    }
    STAMP(ws, 0, 1);
    const int rt = 2 * xcd + (idx >> 4), ct = idx & 15;
    gemm_small(lds, xA, wqA, ct == 0 ? wkA : wvA, rt, ct, ws);
    STAMP(ws, 0, 2);
}

DI float rowmax32(const f32x16& p0, const f32x16& p1) {
    float a = fmaxf(fmaxf(p0[0], p0[1]), p1[0]), b = fmaxf(fmaxf(p0[2], p0[3]), p1[1]); a = fmaxf(fmaxf(a, p1[2]), p1[3]);
#pragma unroll
    for (int r = 4; r < 16; r += 4) { a = fmaxf(fmaxf(a, p0[r]), p0[r + 1]); b = fmaxf(fmaxf(b, p0[r + 2]), p0[r + 3]); a = fmaxf(fmaxf(a, p1[r]), p1[r + 1]); b = fmaxf(fmaxf(b, p1[r + 2]), p1[r + 3]); }
    return fmaxf(a, b);
}
DI float swap32max(float m) { const auto rr = __builtin_amdgcn_permlane32_swap(__float_as_uint(m), __float_as_uint(m), false, false); return fmaxf(__uint_as_float(rr[0]), __uint_as_float(rr[1])); }
DI float swap32sum(float m) { const auto rr = __builtin_amdgcn_permlane32_swap(__float_as_uint(m), __float_as_uint(m), false, false); return __uint_as_float(rr[0]) + __uint_as_float(rr[1]); }
DI bf16x8 packp(const f32x16& p, int b) { const u32x4 w = {cvtpk(p[b], p[b + 1]), cvtpk(p[b + 2], p[b + 3]), cvtpk(p[b + 4], p[b + 5]), cvtpk(p[b + 6], p[b + 7])}; return __builtin_bit_cast(bf16x8, w); }
DI f32x16 splat16(float v) { f32x16 r; _Pragma("unroll") for (int i = 0; i < 16; ++i) r[i] = v; return r; }

DI void attn_b_merge(ldsp lds, char* ws, f32x16 (&o)[4], float mref, float l, int vslot, int row0, int head, int wid, int lane) {
    const int g = wid >> 2, wq = wid & 3, r32 = lane & 31, hh = lane >> 5, pw = wid ^ 4;
    l = swap32sum(l);
    const float meff = l > 0.f ? mref : -INFINITY;
    ldsp xv = lds + 65536 + vslot * 32768;
    WAIT_L(0); BAR;
    { LAS float* ml = (LAS float*)(xv + wid * 512); ml[lane] = meff; ml[64 + lane] = l; }
    WAIT_L(0); BAR;
    float c1;
    { const LAS float* ml = (const LAS float*)(xv + pw * 512);
      const float m2 = ml[lane], l2 = ml[64 + lane];
      const float ms = fmaxf(meff, m2);
      const float f1 = __builtin_amdgcn_exp2f(meff - ms), f2 = __builtin_amdgcn_exp2f(m2 - ms);
      c1 = f1 / (l * f1 + l2 * f2); }
    WAIT_L(0); BAR;
    {
        ldsp w = (g ? lds + 131072 : xv) + wq * 8192 + lane * 16;
        const f32x16 a = (g ? o[0] : o[2]) * c1, b = (g ? o[1] : o[3]) * c1;
#pragma unroll
        for (int i = 0; i < 4; ++i) { *(LAS f32x4*)(w + i * 1024) = (f32x4){a[4 * i], a[4 * i + 1], a[4 * i + 2], a[4 * i + 3]};
                                      *(LAS f32x4*)(w + 4096 + i * 1024) = (f32x4){b[4 * i], b[4 * i + 1], b[4 * i + 2], b[4 * i + 3]}; }
    }
    WAIT_L(0); BAR;
    {
        ldsp r = (g ? xv : lds + 131072) + wq * 8192 + lane * 16;
        char* orow = ws + WS_CTX + (size_t)(row0 + 32 * wq + r32) * 8192 + (size_t)head * 256 + g * 128 + hh * 16;
#pragma unroll
        for (int dd = 0; dd < 2; ++dd) {
            const f32x16& mine = g ? o[2 + dd] : o[dd];
            unsigned w[8];
#pragma unroll
            for (int i = 0; i < 4; ++i) { const f32x4 x = *(const LAS f32x4*)(r + dd * 4096 + i * 1024);
                w[2 * i] = cvtpk(mine[4 * i] * c1 + x[0], mine[4 * i + 1] * c1 + x[1]);
                w[2 * i + 1] = cvtpk(mine[4 * i + 2] * c1 + x[2], mine[4 * i + 3] * c1 + x[3]); }
#pragma unroll
            for (int k = 0; k < 4; k += 2) {
                const auto x0 = __builtin_amdgcn_permlane32_swap(w[2 * k], w[2 * k + 2], false, false);
                const auto x1 = __builtin_amdgcn_permlane32_swap(w[2 * k + 1], w[2 * k + 3], false, false);
                *(u32x4*)(orow + dd * 64 + k * 16) = (u32x4){x0[0], x1[0], x0[1], x1[1]};
            }
        }
    }
}

DI void attn_b(ldsp lds, char* ws, int head, int j) {
    const int tid = threadIdx.x, wid = rfl(tid >> 6), lane = tid & 63, g = wid >> 2, wq = wid & 3, r32 = lane & 31, hh = lane >> 5;
    const char* Kg = ws + WS_KB + (size_t)head * (512u << 10) + (size_t)wid * 4096;
    const char* Vg = ws + WS_VB + (size_t)head * (512u << 10) + (size_t)wid * 4096;
    const char* Qg = ws + WS_QB + (size_t)head * (512u << 10) + lane * 16;
    const unsigned l16 = lane * 16, l0 = ldsaddr_of(lds) + wid * 4096;
    const int NA = 16 - j;
    ldsp kbase = lds + g * 16384 + lane * 16, vbase = lds + 65536 + g * 16384 + lane * 16;
#define KT(x) ((x) < NA ? (x) : ((x) > 16 ? 0 : (x) - NA))
#define KPIECE(x, i) glds16(Kg + (size_t)KT(x) * 32768 + (i) * 1024, l16, l0 + ((x) & 1) * 32768 + (i) * 1024)
#define VPIECE(x, i) glds16(Vg + (size_t)KT(x) * 32768 + (i) * 1024, l16, l0 + 65536 + ((x) & 1) * 32768 + (i) * 1024)
#define KFR(x, h, i) ldsr(kbase + ((x) & 1) * 32768 + (h) * 8192 + (i) * 1024)
#define VFR(x, h, i) ldsr(vbase + ((x) & 1) * 32768 + (h) * 8192 + (i) * 1024)
    bf16x8 qf[8], kf[8], vf[8];
    TSDECL;
#pragma unroll
    for (int i = 0; i < 4; ++i) KPIECE(0, i);
#pragma unroll
    for (int i = 0; i < 4; ++i) VPIECE(0, i);
#pragma unroll
    for (int i = 0; i < 4; ++i) KPIECE(1, i);
#pragma unroll
    for (int s = 0; s < 8; ++s) qf[s] = *(const bf16x8*)(Qg + (size_t)(((15 - j) * 4 + wq) * 8 + s) * 1024);
    asm volatile("" :: "v"(qf[0]), "v"(qf[1]), "v"(qf[2]), "v"(qf[3]), "v"(qf[4]), "v"(qf[5]), "v"(qf[6]), "v"(qf[7]));
    WAIT_V(0); BAR;
    f32x16 o[4]; o[0] = splat16(0.f); o[1] = o[0]; o[2] = o[0]; o[3] = o[0];
    f32x16 sA, sB, negm;
    float mref, l = 0.f;
    {
#pragma unroll
        for (int i = 0; i < 8; ++i) kf[i] = KFR(0, 0, i);
        sA = splat16(0.f);
#pragma unroll
        for (int i = 0; i < 8; ++i) sA = MFMA32(kf[i], qf[i], sA);
#pragma unroll
        for (int i = 0; i < 4; ++i) kf[i] = KFR(0, 1, i);
        float m = fmaxf(sA[0], sA[1]);
#pragma unroll
        for (int r = 2; r < 16; r += 2) m = fmaxf(fmaxf(m, sA[r]), sA[r + 1]);
        mref = swap32max(m);
        sA -= mref; negm = splat16(-mref); asm volatile("" : "+v"(negm));
    }
    u32x4 pw0, pw1;
    TS(0);
#define EXPL(A, e) do { A[e] = __builtin_amdgcn_exp2f(A[e]); l += A[e]; } while (0)
#define SM2(A, e, PW, w) do { EXPL(A, e); EXPL(A, (e) + 1); PW[w] = cvtpk(A[e], A[(e) + 1]); asm volatile("" : "+v"(PW), "+v"(l)); } while (0)
#define PK(A, e, PW, w) do { PW[w] = cvtpk(A[e], A[(e) + 1]); asm volatile("" : "+v"(PW), "+v"(l)); } while (0)
#define PINL asm volatile("" : "+v"(l))
#define FENCE do { asm volatile("" ::: "memory"); __builtin_amdgcn_sched_barrier(0); } while (0)
#define PF(k) __builtin_bit_cast(bf16x8, pw##k)
#define BARRIER_B asm volatile("s_waitcnt vmcnt(0) lgkmcnt(0)\n\ts_barrier" ::: "memory")
#define MX(a, b) do { ma = fmaxf(fmaxf(ma, a), b); asm volatile("" : "+v"(ma)); } while (0)
#define HSTEP(A, B, u, H, QRL, DEC) do { \
        const int xn = (u) + (H), hn = (H) ^ 1;                \
        kf[4] = KFR(xn, hn, 4); B = MFMA32(kf[0], qf[0], negm); SM2(A, 0, pw0, 0);  FENCE; \
        kf[5] = KFR(xn, hn, 5); B = MFMA32(kf[1], qf[1], B);    EXPL(A, 2); PINL; FENCE; \
        kf[6] = KFR(xn, hn, 6); B = MFMA32(kf[2], qf[2], B);    EXPL(A, 3); PK(A, 2, pw0, 1); FENCE; \
        kf[7] = KFR(xn, hn, 7); B = MFMA32(kf[3], qf[3], B);    EXPL(A, 4); PINL; FENCE; \
        if (H) VPIECE((u) + 1, 0); B = MFMA32(kf[4], qf[4], B); EXPL(A, 5); PK(A, 4, pw0, 2); FENCE; \
        if (H) VPIECE((u) + 1, 1); B = MFMA32(kf[5], qf[5], B); SM2(A, 6, pw0, 3); FENCE; \
        TS(1); if (!(H)) { BARRIER_B; FENCE; TS(2); } \
        vf[0] = VFR(u, H, 0); vf[1] = VFR(u, H, 1); B = MFMA32(kf[6], qf[6], B); EXPL(A, 8); PINL; FENCE; \
        vf[2] = VFR(u, H, 2); vf[3] = VFR(u, H, 3); B = MFMA32(kf[7], qf[7], B); EXPL(A, 9); PK(A, 8, pw1, 0); FENCE; \
        TS(3); \
        if (QRL) {      \
            const char* qn = ws + WS_QB + (size_t)head * (512u << 10) + (size_t)(j * 4 + wq) * 8192; \
            asm volatile("global_load_dwordx4 %0, %8, %9\n\tglobal_load_dwordx4 %1, %8, %9 offset:1024\n\tglobal_load_dwordx4 %2, %8, %9 offset:2048\n\tglobal_load_dwordx4 %3, %8, %9 offset:3072\n\t" \
                         "global_load_dwordx4 %4, %8, %10\n\tglobal_load_dwordx4 %5, %8, %10 offset:1024\n\tglobal_load_dwordx4 %6, %8, %10 offset:2048\n\tglobal_load_dwordx4 %7, %8, %10 offset:3072" \
                         : "+v"(qf[0]), "+v"(qf[1]), "+v"(qf[2]), "+v"(qf[3]), "+v"(qf[4]), "+v"(qf[5]), "+v"(qf[6]), "+v"(qf[7]) : "v"(l16), "s"(qn), "s"(qn + 4096) : "memory"); \
        } \
        vf[4] = VFR(u, H, 4); vf[5] = VFR(u, H, 5); o[0] = MFMA32(vf[0], PF(0), o[0]); if (H) VPIECE((u) + 1, 2); else KPIECE((u) + 2, 0); SM2(A, 10, pw1, 1); FENCE; \
        vf[6] = VFR(u, H, 6); vf[7] = VFR(u, H, 7); o[1] = MFMA32(vf[1], PF(0), o[1]); if (H) VPIECE((u) + 1, 3); else KPIECE((u) + 2, 1); SM2(A, 12, pw1, 2); FENCE; \
        o[2] = MFMA32(vf[2], PF(0), o[2]); if (!(H)) KPIECE((u) + 2, 2); EXPL(A, 14); PINL; FENCE; \
        o[3] = MFMA32(vf[3], PF(0), o[3]); if (!(H)) KPIECE((u) + 2, 3); EXPL(A, 15); PK(A, 14, pw1, 3); FENCE; \
        if (xn == NA - 1 || xn == 16) { \
            const int dq = 32 * wq + r32 - 64 * g - 32 * hn - 4 * hh; \
            _Pragma("unroll") for (int r = 0; r < 16; ++r) { const int c = (r & 3) + 8 * (r >> 2); if (c > dq) B[r] = -INFINITY; } \
        } \
        float ma = B[0]; \
        kf[0] = KFR((u) + 1, H, 0); o[0] = MFMA32(vf[4], PF(1), o[0]); MX(B[1], B[2]); MX(B[3], B[4]); FENCE; \
        kf[1] = KFR((u) + 1, H, 1); o[1] = MFMA32(vf[5], PF(1), o[1]); MX(B[5], B[6]); MX(B[7], B[8]); FENCE; \
        kf[2] = KFR((u) + 1, H, 2); o[2] = MFMA32(vf[6], PF(1), o[2]); MX(B[9], B[10]); MX(B[11], B[12]); FENCE; \
        kf[3] = KFR((u) + 1, H, 3); o[3] = MFMA32(vf[7], PF(1), o[3]); MX(B[13], B[14]); MX(B[15], B[15]); FENCE; \
        if (QRL) asm volatile("s_waitcnt vmcnt(4)" : "+v"(qf[0]), "+v"(qf[1]), "+v"(qf[2]), "+v"(qf[3]), "+v"(qf[4]), "+v"(qf[5]), "+v"(qf[6]), "+v"(qf[7]) :: "memory"); \
        TS(4); \
        mlast = ma; \
        if (DEC) { if (__any(ma > THR)) { \
            const float dl = fmaxf(swap32max(ma), 0.f), f = __builtin_amdgcn_exp2f(-dl); \
            mref += dl; l *= f; B -= dl; negm -= dl; \
            _Pragma("unroll") for (int d = 0; d < 4; ++d) o[d] *= f; \
        } } \
        TS(5); \
    } while (0)
    float mlast;
    int u = 0;
    for (; u < NA - 1; ++u) {
        HSTEP(sA, sB, u, 0, false, true);
        HSTEP(sB, sA, u, 1, false, true);
    }
    HSTEP(sA, sB, u, 0, true, true);
    negm = splat16(0.f); asm volatile("" : "+v"(negm));
    HSTEP(sB, sA, u, 1, false, false);
    attn_b_merge(lds, ws, o, mref, l, u & 1, (15 - j) * 128, head, wid, lane);
    o[0] = splat16(0.f); o[1] = o[0]; o[2] = o[0]; o[3] = o[0]; l = 0.f;
    mref = swap32max(mlast); if (mref == -INFINITY) mref = 0.f;
    sA -= mref; negm = splat16(-mref); asm volatile("" : "+v"(negm));
    for (++u; u < 16; ++u) {
        HSTEP(sA, sB, u, 0, false, true);
        HSTEP(sB, sA, u, 1, false, true);
    }
    HSTEP(sA, sB, 16, 0, false, true);
    {
#pragma unroll
        for (int i = 0; i < 8; ++i) vf[i] = VFR(16, 1, i);
#pragma unroll
        for (int e = 0; e < 8; e += 2) SM2(sB, e, pw0, e >> 1);
#pragma unroll
        for (int e = 8; e < 16; e += 2) SM2(sB, e, pw1, (e - 8) >> 1);
#pragma unroll
        for (int d = 0; d < 4; ++d) o[d] = MFMA32(vf[d], PF(0), o[d]);
#pragma unroll
        for (int d = 0; d < 4; ++d) o[d] = MFMA32(vf[4 + d], PF(1), o[d]);
        WAIT_V(0);
        attn_b_merge(lds, ws, o, mref, l, 0, j * 128, head, wid, lane);
        WAIT_L(0);
        TS(6);
    }
    TSOUT(ws, 0);
#undef KT
#undef KPIECE
#undef VPIECE
#undef KFR
#undef VFR
#undef EXPL
#undef SM2
#undef PF
#undef MX
#undef HSTEP
#undef PK
#undef PINL
#undef FENCE
}

DI void attn_a(ldsp lds, char* ws, int qb, int hg) {
    const int tid = threadIdx.x, wid = rfl(tid >> 6), lane = tid & 63, r32 = lane & 31, hh = lane >> 5;
    const int head = hg * 8 + wid;
    const char* Kg = ws + WS_KA + (size_t)wid * 2048;
    const char* Vg = ws + WS_VA + (size_t)wid * 2048;
    const unsigned l16 = lane * 16, l0 = ldsaddr_of(lds) + wid * 2048;
    constexpr float SHIFT = 40.f;
#define ASTAGE(c) do { const unsigned _b = l0 + ((c) & 3) * 32768; const size_t _o = (size_t)((c) & 7) * 16384; \
        glds16(Kg + _o, l16, _b); glds16(Kg + _o + 1024, l16, _b + 1024); glds16(Vg + _o, l16, _b + 16384); glds16(Vg + _o + 1024, l16, _b + 16384 + 1024); } while (0)
#define AKF(i, u) ldsr(lds + l16 + (((i) >> 2) & 3) * 32768 + ((i) & 3) * 4096 + (u) * 1024)
#define AVF(i, u) ldsr(lds + l16 + 16384 + (((i) >> 2) & 3) * 32768 + ((i) & 3) * 4096 + (u) * 1024)
    BAR;
    ASTAGE(0); ASTAGE(1);
    bf16x8 qf[2];
#pragma unroll
    for (int s = 0; s < 2; ++s) qf[s] = *(const bf16x8*)(ws + WS_QA + (size_t)((head * 64 + qb) * 2 + s) * 1024 + lane * 16);
    asm volatile("" :: "v"(qf[0]), "v"(qf[1]));
    WAIT_V(0); BAR;
    const bf16x8 ones = {0x3F80, 0x3F80, 0x3F80, 0x3F80, 0x3F80, 0x3F80, 0x3F80, 0x3F80};
    f32x16 o = splat16(0.f), ls = splat16(0.f), negm, sA0, sA1, sB0, sB1;
    bf16x8 kf[4], vf[4];
    u32x4 pa0, pa1, pa2, pa3, pb0, pb1, pb2, pb3;
    float mref;
    {
#pragma unroll
        for (int u = 0; u < 4; ++u) kf[u] = AKF(0, u);
        sA0 = MFMA32(kf[0], qf[0], o); sA1 = MFMA32(kf[2], qf[0], o); sA0 = MFMA32(kf[1], qf[1], sA0); sA1 = MFMA32(kf[3], qf[1], sA1);
#pragma unroll
        for (int u = 0; u < 4; ++u) kf[u] = AKF(1, u);
        mref = swap32max(rowmax32(sA0, sA1));
        negm = splat16(-(mref + SHIFT)); asm volatile("" : "+v"(negm));
        sA0 += negm; sA1 += negm;
        pb0 = (u32x4){0, 0, 0, 0}; pb1 = pb0; pb2 = pb0; pb3 = pb0;
#pragma unroll
        for (int u = 0; u < 4; ++u) vf[u] = __builtin_bit_cast(bf16x8, pb0);
    }
#define FENCE do { asm volatile("" ::: "memory"); __builtin_amdgcn_sched_barrier(0); } while (0)
#define EX2(A, e, PW, w) do { A[e] = __builtin_amdgcn_exp2f(A[e]); A[(e) + 1] = __builtin_amdgcn_exp2f(A[(e) + 1]); PW[w] = cvtpk(A[e], A[(e) + 1]); asm volatile("" : "+v"(PW)); } while (0)
#define BC(x) __builtin_bit_cast(bf16x8, x)
#define ASTEP(A0, A1, B0, B1, PA0, PA1, PA2, PA3, PB0, PB1, PB2, PB3, i) do { \
        B0 = MFMA32(kf[0], qf[0], negm);  EX2(A0, 0, PA0, 0);  FENCE; \
        B1 = MFMA32(kf[2], qf[0], negm);  EX2(A0, 2, PA0, 1);  FENCE; \
        B0 = MFMA32(kf[1], qf[1], B0);    EX2(A0, 4, PA0, 2);  EX2(A0, 6, PA0, 3); FENCE; \
        B1 = MFMA32(kf[3], qf[1], B1);    EX2(A0, 8, PA1, 0);  FENCE; \
        kf[0] = AKF((i) + 2, 0); kf[1] = AKF((i) + 2, 1); \
        o = MFMA32(vf[0], BC(PB0), o);    EX2(A0, 10, PA1, 1); FENCE; \
        kf[2] = AKF((i) + 2, 2); kf[3] = AKF((i) + 2, 3); \
        ls = MFMA32(ones, BC(PB0), ls);   EX2(A0, 12, PA1, 2); EX2(A0, 14, PA1, 3); FENCE; \
        o = MFMA32(vf[1], BC(PB1), o);    EX2(A1, 0, PA2, 0);  FENCE; \
        ls = MFMA32(ones, BC(PB1), ls);   EX2(A1, 2, PA2, 1);  FENCE; \
        o = MFMA32(vf[2], BC(PB2), o);    EX2(A1, 4, PA2, 2);  EX2(A1, 6, PA2, 3); FENCE; \
        vf[0] = AVF(i, 0); vf[1] = AVF(i, 1); \
        ls = MFMA32(ones, BC(PB2), ls);   EX2(A1, 8, PA3, 0);  FENCE; \
        o = MFMA32(vf[3], BC(PB3), o);    EX2(A1, 10, PA3, 1); FENCE; \
        vf[2] = AVF(i, 2); vf[3] = AVF(i, 3); \
        ls = MFMA32(ones, BC(PB3), ls);   EX2(A1, 12, PA3, 2); EX2(A1, 14, PA3, 3); FENCE; \
        const u32x4 ov = (PA0 | PA1) | (PA2 | PA3); \
        if (__any(((ov[0] | ov[1] | ov[2] | ov[3]) & 0x40004000u) != 0u)) {         \
            const f32x16 z = splat16(0.f); \
            A0 = MFMA32(AKF(i, 0), qf[0], z); A1 = MFMA32(AKF(i, 2), qf[0], z); A0 = MFMA32(AKF(i, 1), qf[1], A0); A1 = MFMA32(AKF(i, 3), qf[1], A1); \
            const float mn = fmaxf(swap32max(rowmax32(A0, A1)), mref), dl = mn - mref, f = __builtin_amdgcn_exp2f(-dl); \
            mref = mn; o *= f; ls *= f; negm -= dl; B0 -= dl; B1 -= dl; A0 += negm; A1 += negm; \
            EX2(A0, 0, PA0, 0); EX2(A0, 2, PA0, 1); EX2(A0, 4, PA0, 2); EX2(A0, 6, PA0, 3); EX2(A0, 8, PA1, 0); EX2(A0, 10, PA1, 1); EX2(A0, 12, PA1, 2); EX2(A0, 14, PA1, 3); \
            EX2(A1, 0, PA2, 0); EX2(A1, 2, PA2, 1); EX2(A1, 4, PA2, 2); EX2(A1, 6, PA2, 3); EX2(A1, 8, PA3, 0); EX2(A1, 10, PA3, 1); EX2(A1, 12, PA3, 2); EX2(A1, 14, PA3, 3); \
        } \
    } while (0)
    for (int c = 0; c < 8; ++c) {
        asm volatile("s_waitcnt vmcnt(0) lgkmcnt(0)\n\ts_barrier" ::: "memory");
        ASTAGE(c + 2);
        FENCE;
        const int i0 = c * 4;
        ASTEP(sA0, sA1, sB0, sB1, pa0, pa1, pa2, pa3, pb0, pb1, pb2, pb3, i0);
        ASTEP(sB0, sB1, sA0, sA1, pb0, pb1, pb2, pb3, pa0, pa1, pa2, pa3, i0 + 1);
        ASTEP(sA0, sA1, sB0, sB1, pa0, pa1, pa2, pa3, pb0, pb1, pb2, pb3, i0 + 2);
        ASTEP(sB0, sB1, sA0, sA1, pb0, pb1, pb2, pb3, pa0, pa1, pa2, pa3, i0 + 3);
    }
    o = MFMA32(vf[0], BC(pb0), o); ls = MFMA32(ones, BC(pb0), ls); o = MFMA32(vf[1], BC(pb1), o); ls = MFMA32(ones, BC(pb1), ls);
    o = MFMA32(vf[2], BC(pb2), o); ls = MFMA32(ones, BC(pb2), ls); o = MFMA32(vf[3], BC(pb3), o); ls = MFMA32(ones, BC(pb3), ls);
#undef ASTAGE
#undef AKF
#undef AVF
#undef FENCE
#undef EX2
#undef BC
#undef ASTEP
    const float inv = 1.f / ls[0];
    unsigned w[8];
#pragma unroll
    for (int i = 0; i < 4; ++i) { w[2 * i] = cvtpk(o[4 * i] * inv, o[4 * i + 1] * inv); w[2 * i + 1] = cvtpk(o[4 * i + 2] * inv, o[4 * i + 3] * inv); }
    char* orow = ws + WS_OA + (size_t)(qb * 32 + r32) * 2048 + (size_t)head * 64 + hh * 16;
#pragma unroll
    for (int k = 0; k < 4; k += 2) {
        const auto x0 = __builtin_amdgcn_permlane32_swap(w[2 * k], w[2 * k + 2], false, false);
        const auto x1 = __builtin_amdgcn_permlane32_swap(w[2 * k + 1], w[2 * k + 3], false, false);
        *(u32x4*)(orow + k * 16) = (u32x4){x0[0], x1[0], x0[1], x1[1]};
    }
    WAIT_V(0);
}

__global__ __launch_bounds__(512, 2) void k_attn(char* __restrict__ ws, const char* __restrict__ woA, const char* __restrict__ woB) {
    extern __shared__ __attribute__((aligned(16))) unsigned char smem[];
    ldsp lds = (ldsp)smem;
    const int b = blockIdx.x, xcd = b & 7, idx = b >> 3;
    STAMP(ws, 1, 0);
    warm(woB + (size_t)b * 32768, 4, ldsaddr_of(lds) + 131072);
    warm(woA + (size_t)b * 8192, 1, ldsaddr_of(lds) + 131072);
    attn_b(lds, ws, xcd * 4 + (idx >> 3), idx & 7);
    STAMP(ws, 1, 1);
    attn_a(lds, ws, b >> 2, b & 3);
    STAMP(ws, 1, 2);
}

DI void gemm_mid(ldsp lds, const char* gA, unsigned lda2, const char* gW, int nsteps, f32x4 (&acc)[4][2], char* ws = nullptr) {
    TSDECL;
    const int tid = threadIdx.x, wid = rfl(tid >> 6), lane = tid & 63, wr = wid >> 2, wc = wid & 3, fr = lane & 15, fq = lane >> 4;
    constexpr unsigned ldw2 = 2048;
    unsigned voffA[2], voffW[2];
#pragma unroll
    for (int i = 0; i < 2; ++i) {
        voffA[i] = (unsigned)((tid >> 3) + 64 * i) * lda2 + (unsigned)(((tid & 7) ^ ((tid >> 3) & 7)) << 4);
        const int kg = (tid >> 7) + 4 * i, ng = (tid >> 5) & 3, r = (tid >> 2) & 7, c = (tid & 3) ^ (((tid >> 7) & 1) << 1);
        voffW[i] = (unsigned)(kg * 8 + r) * ldw2 + (unsigned)(ng * 64 + c * 16);
    }
    const int aoff0 = (wr * 64 + fr) * 128 + ((fq ^ (fr & 7)) << 4), aoff1 = aoff0 ^ 64;
    const int boff0 = 16384 + (fq * 4 + wc) * 512 + ((lane >> 2) & 3) * 64 + (((lane & 3) * 8) ^ ((fq & 1) << 5)), boff1 = boff0 ^ 32;
#define MPIECE(t, i) do { const int _t = (t) < nsteps ? (t) : nsteps - 1; const unsigned _b = ldsaddr_of(lds) + ((t) & 3) * 32768 + wid * 1024; \
        if ((i) < 2) glds16(gA + (size_t)_t * 128, voffA[(i) & 1], _b + ((i) & 1) * 8192); \
        else glds16(gW + (size_t)_t * 64 * ldw2, voffW[(i) & 1], _b + 16384 + ((i) & 1) * 8192); } while (0)
#define MFENCE do { asm volatile("" ::: "memory"); __builtin_amdgcn_sched_barrier(0); } while (0)
#define MRDA(AF, t, m) do { ldsp _b = lds + ((t) & 3) * 32768; AF[m][0] = ldsr(_b + aoff0 + (m) * 2048); AF[m][1] = ldsr(_b + aoff1 + (m) * 2048); } while (0)
#define MRDB(BF, t, k) do { ldsp _b = lds + ((t) & 3) * 32768; BF[0][k] = ldstr2(_b + boff0 + (k) * 8192); BF[1][k] = ldstr2(_b + boff1 + (k) * 8192); } while (0)
#define MM4(AF, BF, k, m0) do { acc[m0][0] = MFMA16(BF[0][k], AF[m0][k], acc[m0][0]); acc[m0][1] = MFMA16(BF[1][k], AF[m0][k], acc[m0][1]); \
        acc[(m0) + 1][0] = MFMA16(BF[0][k], AF[(m0) + 1][k], acc[(m0) + 1][0]); acc[(m0) + 1][1] = MFMA16(BF[1][k], AF[(m0) + 1][k], acc[(m0) + 1][1]); } while (0)
#define MSTEP(AX, BX, AY, BY, t) do { \
        TS(0); asm volatile("s_waitcnt vmcnt(4) lgkmcnt(0)" ::: "memory"); TS(1); asm volatile("s_barrier" ::: "memory"); TS(2); MFENCE; \
        MRDB(BY, (t) + 1, 0); MRDA(AY, (t) + 1, 0); MM4(AX, BX, 0, 0); MPIECE((t) + 3, 0); MFENCE; \
        MRDA(AY, (t) + 1, 1); MRDA(AY, (t) + 1, 2); MM4(AX, BX, 0, 2); MPIECE((t) + 3, 1); MFENCE; \
        MRDB(BY, (t) + 1, 1); MRDA(AY, (t) + 1, 3); MM4(AX, BX, 1, 0); MPIECE((t) + 3, 2); MFENCE; \
        MM4(AX, BX, 1, 2); MPIECE((t) + 3, 3); MFENCE; \
    } while (0)
    bf16x8 A0[4][2], B0[2][2], A1[4][2], B1[2][2];
#pragma unroll
    for (int i = 0; i < 4; ++i) MPIECE(0, i);
#pragma unroll
    for (int i = 0; i < 4; ++i) MPIECE(1, i);
#pragma unroll
    for (int i = 0; i < 4; ++i) MPIECE(2, i);
    WAIT_V(8); BAR;
    MRDB(B0, 0, 0); MRDB(B0, 0, 1); MRDA(A0, 0, 0); MRDA(A0, 0, 1); MRDA(A0, 0, 2); MRDA(A0, 0, 3);
    for (int t = 0; t < nsteps; t += 2) {
        MSTEP(A0, B0, A1, B1, t);
        MSTEP(A1, B1, A0, B0, t + 1);
    }
#undef MPIECE
#undef MFENCE
#undef MRDA
#undef MRDB
#undef MM4
#undef MSTEP
    TS(0);
    if (ws) TSOUT(ws, 2);
    WAIT_V(0); WAIT_L(0); BAR;
}

DI void store_out(char* outp  , const f32x4 (&acc)[4][2]) {
    const int tid = threadIdx.x, wid = rfl(tid >> 6), lane = tid & 63, wr = wid >> 2, wc = wid & 3, fr = lane & 15, fq = lane >> 4;
    const int c8 = (fq >> 1) | ((fq & 1) << 1);
#pragma unroll
    for (int m = 0; m < 4; ++m) {
        const f32x4 v0 = acc[m][0], v1 = acc[m][1];
        const auto r0 = __builtin_amdgcn_permlane16_swap(cvtpk(v0[0], v0[1]), cvtpk(v1[0], v1[1]), false, false);
        const auto r1 = __builtin_amdgcn_permlane16_swap(cvtpk(v0[2], v0[3]), cvtpk(v1[2], v1[3]), false, false);
        *(u32x4*)(outp + (size_t)(wr * 64 + m * 16 + fr) * 4096 + (size_t)(wc * 64 + c8 * 16)) = (u32x4){r0[0], r1[0], r0[1], r1[1]};
    }
}

constexpr int SPLIT0 = 40;
__global__ __launch_bounds__(512, 2) void k_oproj(const char* __restrict__ woA, const char* __restrict__ woB, char* __restrict__ ws, char* __restrict__ out) {
    extern __shared__ __attribute__((aligned(16))) unsigned char smem[];
    ldsp lds = (ldsp)smem;
    const int b = blockIdx.x, xcd = b & 7, idx = b >> 3, pi = idx >> 1, role = idx & 1;
    const int rt = 4 * (xcd >> 1) + (pi & 3), ct = 4 * (xcd & 1) + (pi >> 2), tile = rt * 8 + ct;
    const int tid = threadIdx.x;
    f32x4 acc[4][2];
#pragma unroll
    for (int m = 0; m < 4; ++m)
#pragma unroll
        for (int n = 0; n < 2; ++n) acc[m][n] = (f32x4){0.f, 0.f, 0.f, 0.f};
    const char* ctx = ws + WS_CTX + (size_t)rt * 128 * 8192;
    unsigned* flag = (unsigned*)(ws + WS_FLAG + tile * 128);
    char* part = ws + WS_PART + (size_t)tile * 65536;
    STAMP(ws, 2, 0);
    if (role == 1) {
        gemm_mid(lds, ctx + SPLIT0 * 128, 8192u, woB + (size_t)SPLIT0 * 64 * 2048 + ct * 256, 64 - SPLIT0, acc, ws);
        const __amdgpu_buffer_rsrc_t rs = __builtin_amdgcn_make_buffer_rsrc(part, 0, 65536, 0x00020000);
#pragma unroll
        for (int m = 0; m < 4; ++m)
#pragma unroll
            for (int n = 0; n < 2; ++n) __builtin_amdgcn_raw_buffer_store_b128(__builtin_bit_cast(u32x4, acc[m][n]), rs, (m * 2 + n) * 8192 + tid * 16, 0, 16);
        WAIT_V(0);
        __syncthreads();
        if (tid == 0) __hip_atomic_store(flag, 1u, __ATOMIC_RELAXED, __HIP_MEMORY_SCOPE_AGENT);
        STAMP(ws, 2, 1);
#pragma unroll
        for (int m = 0; m < 4; ++m)
#pragma unroll
            for (int n = 0; n < 2; ++n) acc[m][n] = (f32x4){0.f, 0.f, 0.f, 0.f};
        gemm_mid(lds, ws + WS_OA + (size_t)rt * 128 * 2048, 2048u, woA + ct * 256, 16, acc);
        store_out(out + (size_t)rt * 128 * 4096 + ct * 256, acc);
        STAMP(ws, 2, 2);
    } else {
        gemm_mid(lds, ctx, 8192u, woB + ct * 256, SPLIT0, acc, ws);
        STAMP(ws, 2, 1);
        if (tid < 64) {
            while (rfl((int)__hip_atomic_load(flag, __ATOMIC_RELAXED, __HIP_MEMORY_SCOPE_AGENT)) != 1) __builtin_amdgcn_s_sleep(2);
            __builtin_amdgcn_fence(__ATOMIC_ACQUIRE, "agent");
            WAIT_V(0);
        }
        __syncthreads();
        const __amdgpu_buffer_rsrc_t rs = __builtin_amdgcn_make_buffer_rsrc(part, 0, 65536, 0x00020000);
#pragma unroll
        for (int m = 0; m < 4; ++m)
#pragma unroll
            for (int n = 0; n < 2; ++n) acc[m][n] += __builtin_bit_cast(f32x4, __builtin_amdgcn_raw_buffer_load_b128(rs, (m * 2 + n) * 8192 + tid * 16, 0, 16));
        store_out(out + (size_t)rt * 128 * 4096 + 2048 + ct * 256, acc);
        STAMP(ws, 2, 2);
    }
}

extern "C" void kernel_launch(void* const* d_in, const int* in_sizes, int n_in, void* d_out, int out_size, void* d_ws, size_t ws_size, hipStream_t stream) {
    (void)in_sizes; (void)n_in; (void)out_size; (void)ws_size;
    constexpr int LDS1 = 131072 + 8192, LDS2 = 163840, LDS3 = 131072;
    (void)hipFuncSetAttribute((const void*)k_proj, hipFuncAttributeMaxDynamicSharedMemorySize, LDS1);
    (void)hipFuncSetAttribute((const void*)k_attn, hipFuncAttributeMaxDynamicSharedMemorySize, LDS2);
    (void)hipFuncSetAttribute((const void*)k_oproj, hipFuncAttributeMaxDynamicSharedMemorySize, LDS3);
    char* ws = (char*)d_ws;
    k_proj<<<256, 512, LDS1, stream>>>((const char*)d_in[0], (const char*)d_in[1], (const char*)d_in[2], (const char*)d_in[3], (const char*)d_in[5],
                                       (const char*)d_in[6], (const char*)d_in[7], (const char*)d_in[8], (const char*)d_in[9], ws);
    k_attn<<<256, 512, LDS2, stream>>>(ws, (const char*)d_in[4], (const char*)d_in[10]);
    k_oproj<<<256, 512, LDS3, stream>>>((const char*)d_in[4], (const char*)d_in[10], ws, (char*)d_out);
}
```

```cpp
#include <hip/hip_runtime.h>
#include <stdint.h>

#define LAS __attribute__((address_space(3)))
#define DI __device__ __forceinline__
typedef unsigned short u16;
typedef short bf16x8 __attribute__((ext_vector_type(8)));
typedef short s16x4 __attribute__((ext_vector_type(4)));
typedef float f32x4 __attribute__((ext_vector_type(4)));
typedef float f32x16 __attribute__((ext_vector_type(16)));
typedef unsigned u32x4 __attribute__((ext_vector_type(4)));
typedef unsigned u32x2 __attribute__((ext_vector_type(2)));
typedef LAS unsigned char* ldsp;

constexpr size_t WS_QB = 0;
constexpr size_t WS_KB = WS_QB + (16u << 20);
constexpr size_t WS_VB = WS_KB + (16u << 20);
constexpr size_t WS_QA = WS_VB + (16u << 20);
constexpr size_t WS_KA = WS_QA + (4u << 20);
constexpr size_t WS_VA = WS_KA + (128u << 10);
constexpr size_t WS_CTX = WS_VA + (128u << 10);
constexpr size_t WS_OA = WS_CTX + (16u << 20);
constexpr size_t WS_PART = WS_OA + (4u << 20);
constexpr size_t WS_FLAG = WS_PART + (8u << 20);

constexpr float LOG2E = 1.4426950408889634f;
constexpr float SCALE_A = 0.17677669529663687f * LOG2E;
constexpr float SCALE_B = 0.08838834764831845f * LOG2E;
constexpr float THR = 40.f;

#define WAIT_V(n) asm volatile("s_waitcnt vmcnt(" #n ")" ::: "memory")
#define WAIT_L(n) asm volatile("s_waitcnt lgkmcnt(" #n ")" ::: "memory")
#define BAR __builtin_amdgcn_s_barrier()
#define SCHED __builtin_amdgcn_sched_barrier(0)
#define MFMA16(a, b, c) __builtin_amdgcn_mfma_f32_16x16x32_bf16(a, b, c, 0, 0, 0)
#define MFMA32(a, b, c) __builtin_amdgcn_mfma_f32_32x32x16_bf16(a, b, c, 0, 0, 0)

typedef float f32x2 __attribute__((ext_vector_type(2)));
typedef __bf16 bf16x2_t __attribute__((ext_vector_type(2)));
DI unsigned cvtpk(float lo, float hi) { const f32x2 v = {lo, hi}; return __builtin_bit_cast(unsigned, __builtin_convertvector(v, bf16x2_t)); }
DI void glds16(const char* sbase, unsigned voff, unsigned ldsaddr) {
    unsigned keep;
    asm volatile("s_mov_b32 %0, m0\n\ts_mov_b32 m0, %3\n\ts_nop 0\n\tglobal_load_lds_dwordx4 %1, %2\n\ts_mov_b32 m0, %0"
                 : "=&s"(keep) : "v"(voff), "s"(sbase), "s"(ldsaddr) : "memory");
}
DI unsigned ldsaddr_of(ldsp p) { return (unsigned)(size_t)p; }
DI bf16x8 ldsr(ldsp p) { return *(const LAS bf16x8*)p; }
DI s16x4 ldstr(ldsp p) { return __builtin_amdgcn_ds_read_tr16_b64_v4i16((LAS s16x4*)p); }
DI bf16x8 ldstr2(ldsp p) { const s16x4 a = ldstr(p), b = ldstr(p + 256); return (bf16x8){a[0], a[1], a[2], a[3], b[0], b[1], b[2], b[3]}; }
DI int rfl(int x) { return __builtin_amdgcn_readfirstlane(x); }
#define STAMP(ws, kern, i) do {} while (0)
#define TSDECL do {} while (0)
#define TS(i) do {} while (0)
#define TSOUT(ws, slot) do {} while (0)
#define KEEP(...) do {} while (0)

DI void warm(const char* base, int npieces, unsigned dump) {
    const int wid = rfl(threadIdx.x >> 6); const unsigned l16 = (threadIdx.x & 63) * 16;
    for (int i = 0; i < npieces; ++i) glds16(base + (size_t)i * 8192 + wid * 1024, l16, dump + wid * 1024);
}

constexpr int HTB = 16384;
#define SA(b, h) (((b) * 2 + (h)) * HTB)
#define SB(b, h) ((4 + (b) * 2 + (h)) * HTB)

template <bool SWAP>
DI void gemm_big(ldsp lds, const char* gA, unsigned lda2, const char* gW, unsigned ldw2, int nt, int nunits,
                 char* out, int row0, int head0, float scale) {
    const int tid = threadIdx.x, wid = rfl(tid >> 6), lane = tid & 63, wr = wid >> 2, wc = wid & 3, fr = lane & 15, fq = lane >> 4;
    unsigned voffA[2], voffW[2];
#pragma unroll
    for (int i = 0; i < 2; ++i) {
        voffA[i] = (unsigned)((tid >> 3) + 64 * i) * lda2 + (unsigned)(((tid & 7) ^ ((tid >> 3) & 7)) << 4);
        const int kg = (tid >> 7) + 4 * i, ng = (tid >> 5) & 3, r = (tid >> 2) & 7, c = (tid & 3) ^ (((tid >> 7) & 1) << 1);
        voffW[i] = (unsigned)(kg * 8 + r) * ldw2 + (unsigned)(ng * 64 + c * 16);
    }
    const size_t kstepA = 128, kstepW = (size_t)64 * ldw2, hstepA = (size_t)128 * lda2, hstepW = 256;
    const unsigned ldsw = ldsaddr_of(lds) + (unsigned)wid * 1024u;
    const int rowb = wr * 64 + (SWAP ? 16 * (fr >> 3) + (fr & 7) : fr);
    const int aoff0 = rowb * 128 + ((fq ^ (fr & 7)) << 4), aoff1 = aoff0 ^ 64;
    const int boff0 = (fq * 4 + wc) * 512 + ((lane >> 2) & 3) * 64 + (((lane & 3) * 8) ^ ((fq & 1) << 5)), boff1 = boff0 ^ 32;
#define MOFF(m) (SWAP ? ((m) >> 1) * 4096 + ((m) & 1) * 1024 : (m) * 2048)
#define STAGE(bufoff, gbase, voff) do { _Pragma("unroll") for (int _i = 0; _i < 2; ++_i) glds16((gbase), (voff)[_i], (bufoff) + ldsw + _i * 8192); } while (0)
#define LDA(dst, b, h) do { _Pragma("unroll") for (int m = 0; m < 4; ++m) { dst[m][0] = ldsr(lds + SA(b, h) + aoff0 + MOFF(m)); dst[m][1] = ldsr(lds + SA(b, h) + aoff1 + MOFF(m)); } } while (0)
#define LDB(dst, b, h) do { _Pragma("unroll") for (int k = 0; k < 2; ++k) { dst[0][k] = ldstr2(lds + SB(b, h) + boff0 + k * 8192); dst[1][k] = ldstr2(lds + SB(b, h) + boff1 + k * 8192); } } while (0)
#define MMA(ai, bj, At, Bt) do { __builtin_amdgcn_s_setprio(1); _Pragma("unroll") for (int m = 0; m < 4; ++m) _Pragma("unroll") for (int n = 0; n < 2; ++n) _Pragma("unroll") for (int k = 0; k < 2; ++k) \
        acc[ai][bj][m][n] = SWAP ? MFMA16(At[m][k], Bt[n][k], acc[ai][bj][m][n]) : MFMA16(Bt[n][k], At[m][k], acc[ai][bj][m][n]); __builtin_amdgcn_s_setprio(0); } while (0)
    f32x4 acc[2][2][4][2];
#pragma unroll
    for (int a = 0; a < 2; ++a)
#pragma unroll
        for (int b = 0; b < 2; ++b)
#pragma unroll
            for (int m = 0; m < 4; ++m)
#pragma unroll
                for (int n = 0; n < 2; ++n) acc[a][b][m][n] = (f32x4){0.f, 0.f, 0.f, 0.f};
    bf16x8 At[4][2], B0[2][2], B1[2][2];
    const char* cA = gA; const char* cW = gW;
    STAGE(SB(0, 0), cW, voffW); STAGE(SA(0, 0), cA, voffA); STAGE(SB(0, 1), cW + hstepW, voffW); STAGE(SA(0, 1), cA + hstepA, voffA);
    if (wr == 1) BAR;
    WAIT_V(4); BAR;
    STAGE(SB(1, 0), cW + kstepW, voffW); STAGE(SA(1, 0), cA + kstepA, voffA); STAGE(SB(1, 1), cW + hstepW + kstepW, voffW);
    WAIT_V(6); BAR;
    for (int ui = 0;; ++ui) {
        const bool has_next = ui + 1 < nunits;
        const char* nW = has_next ? cW + 512 : cW;
        for (int t = 0; t < nt; t += 2) {
            const bool last = (t == nt - 2);
            const char* a1 = cA + (size_t)(t + 1) * kstepA;
            const char* a2 = last ? cA : cA + (size_t)(t + 2) * kstepA; const char* b2 = last ? nW : cW + (size_t)(t + 2) * kstepW;
            const char* a3 = a2 + kstepA; const char* b3 = b2 + kstepW;
            LDB(B0, 0, 0); SCHED; LDA(At, 0, 0); STAGE(SA(1, 1), a1 + hstepA, voffA);
            WAIT_L(8); BAR; WAIT_L(0); MMA(0, 0, At, B0); BAR; SCHED;
            LDB(B1, 0, 1); STAGE(SB(0, 0), b2, voffW);
            BAR; WAIT_L(0); MMA(0, 1, At, B1); BAR;
            LDA(At, 0, 1); STAGE(SA(0, 0), a2, voffA);
            BAR; WAIT_L(0); MMA(1, 0, At, B0); BAR; SCHED;
            STAGE(SB(0, 1), b2 + hstepW, voffW);
            WAIT_V(6); BAR; MMA(1, 1, At, B1); BAR;
            LDB(B0, 1, 0); SCHED; LDA(At, 1, 0); STAGE(SA(0, 1), a2 + hstepA, voffA);
            WAIT_L(8); BAR; WAIT_L(0); MMA(0, 0, At, B0); BAR; SCHED;
            LDB(B1, 1, 1); STAGE(SB(1, 0), b3, voffW);
            BAR; WAIT_L(0); MMA(0, 1, At, B1); BAR;
            LDA(At, 1, 1); STAGE(SA(1, 0), a3, voffA);
            BAR; WAIT_L(0); MMA(1, 0, At, B0); BAR; SCHED;
            STAGE(SB(1, 1), b3 + hstepW, voffW);
            WAIT_V(6); BAR; MMA(1, 1, At, B1); BAR;
        }
        const int h0 = head0 + 2 * ui;
        if constexpr (!SWAP) {
#pragma unroll
            for (int ai = 0; ai < 2; ++ai)
#pragma unroll
                for (int m = 0; m < 4; ++m) {
                    const int t = row0 + ai * 128 + wr * 64 + m * 16 + fr;
#pragma unroll
                    for (int bj = 0; bj < 2; ++bj) {
                        const f32x4 v0 = acc[ai][bj][m][0] * scale, v1 = acc[ai][bj][m][1] * scale;
                        const auto r0 = __builtin_amdgcn_permlane16_swap(cvtpk(v0[0], v0[1]), cvtpk(v1[0], v1[1]), false, false);
                        const auto r1 = __builtin_amdgcn_permlane16_swap(cvtpk(v0[2], v0[3]), cvtpk(v1[2], v1[3]), false, false);
                        const u32x4 w = {r0[0], r1[0], r0[1], r1[1]};
                        const size_t off = ((size_t)(((h0 + bj) * 64 + (t >> 5)) * 8 + 2 * wc + (fq & 1))) * 1024 + (size_t)(((t & 31) + 32 * (fq >> 1)) * 16);
                        *(u32x4*)(out + off) = w;
                    }
                }
        } else {
#pragma unroll
            for (int ai = 0; ai < 2; ++ai)
#pragma unroll
                for (int mp = 0; mp < 2; ++mp) {
                    const int pos = row0 + ai * 128 + wr * 64 + 32 * mp + 8 * fq;
#pragma unroll
                    for (int bj = 0; bj < 2; ++bj)
#pragma unroll
                        for (int n = 0; n < 2; ++n) {
                            const f32x4 a = acc[ai][bj][2 * mp][n], b = acc[ai][bj][2 * mp + 1][n];
                            const u32x4 w = {cvtpk(a[0], a[1]), cvtpk(a[2], a[3]), cvtpk(b[0], b[1]), cvtpk(b[2], b[3])};
                            const size_t off = ((size_t)(((h0 + bj) * 128 + (pos >> 4)) * 4 + wc)) * 1024 + (size_t)((n * 16 + fr + 32 * (fq & 1)) * 16);
                            *(u32x4*)(out + off) = w;
                        }
                }
        }
        if (!has_next) break;
#pragma unroll
        for (int a = 0; a < 2; ++a)
#pragma unroll
            for (int b = 0; b < 2; ++b)
#pragma unroll
                for (int m = 0; m < 4; ++m)
#pragma unroll
                    for (int n = 0; n < 2; ++n) acc[a][b][m][n] = (f32x4){0.f, 0.f, 0.f, 0.f};
        cW = nW;
    }
    WAIT_V(16);
    if (wr == 0) BAR;
    BAR;
#undef MOFF
#undef STAGE
#undef LDA
#undef LDB
#undef MMA
}

constexpr int SG_STAGE = 28672;
DI void gemm_small(ldsp lds, const char* xA, const char* wq, const char* wx  , int rt, int ct, char* ws) {
    const int tid = threadIdx.x, wid = rfl(tid >> 6), lane = tid & 63, wr = wid >> 1, wc = wid & 1, fr = lane & 15, fq = lane >> 4;
    const bool extra = ct < 2;
    const char* gA = xA + (size_t)rt * 128 * 2048;
    const char* gW = wq + (size_t)ct * 128;
    unsigned voffA[2];
#pragma unroll
    for (int i = 0; i < 2; ++i) voffA[i] = (unsigned)((tid >> 3) + 64 * i) * 2048u + (unsigned)(((tid & 7) ^ ((tid >> 3) & 7)) << 4);
    const int cch = (lane & 3) ^ ((wid & 1) << 1);
    const unsigned voffW = (unsigned)(wid * 8 + ((lane >> 2) & 7)) * 2048u + (unsigned)((lane >> 5) * 64 + cch * 16);
    const unsigned voffX = (unsigned)(wid * 8 + ((lane >> 2) & 7)) * 64u + (unsigned)(cch * 16);
    const int aoff0 = (wr * 32 + fr) * 128 + ((fq ^ (fr & 7)) << 4), aoff1 = aoff0 ^ 64;
    const int boff0 = 16384 + (fq * 2 + wc) * 512 + ((lane >> 2) & 3) * 64 + (((lane & 3) * 8) ^ ((fq & 1) << 5)), boff1 = boff0 ^ 32;
    const int xoff = 24576 + fq * 512 + ((lane >> 2) & 3) * 64 + (((wc * 32) + (lane & 3) * 8) ^ ((fq & 1) << 5));
#define SPIECE(t, i) do { const int _t = (t) < 16 ? (t) : 15; const unsigned _b = ldsaddr_of(lds) + ((t) & 3) * SG_STAGE; \
        if ((i) < 2) glds16(gA + _t * 128, voffA[(i) & 1], _b + wid * 1024 + ((i) & 1) * 8192); \
        else if ((i) == 2) glds16(gW + (size_t)_t * 64 * 2048, voffW, _b + 16384 + wid * 1024); \
        else if (extra) { const unsigned _x = (unsigned)rfl((int)(_b + 24576 + wid * 512)); if (lane < 32) glds16(wx + _t * 64 * 64, voffX, _x); } } while (0)
#define SFENCE do { asm volatile("" ::: "memory"); __builtin_amdgcn_sched_barrier(0); } while (0)
#define SRD(AF, BF, XF, t) do { ldsp _b = lds + ((t) & 3) * SG_STAGE; \
        _Pragma("unroll") for (int k = 0; k < 2; ++k) { BF[0][k] = ldstr2(_b + boff0 + k * 4096); BF[1][k] = ldstr2(_b + boff1 + k * 4096); } \
        _Pragma("unroll") for (int m = 0; m < 2; ++m) { AF[m][0] = ldsr(_b + aoff0 + m * 2048); AF[m][1] = ldsr(_b + aoff1 + m * 2048); } \
        if (extra) { XF[0] = ldstr2(_b + xoff); XF[1] = ldstr2(_b + xoff + 2048); } } while (0)
#define SMM(AF, BF, k) do { _Pragma("unroll") for (int m = 0; m < 2; ++m) _Pragma("unroll") for (int n = 0; n < 2; ++n) acc[m][n] = MFMA16(BF[n][k], AF[m][k], acc[m][n]); } while (0)
#define SSTEP(AX, BX, XX, AY, BY, XY, t) do { \
        if (extra) asm volatile("s_waitcnt vmcnt(8) lgkmcnt(0)\n\ts_barrier" ::: "memory"); else asm volatile("s_waitcnt vmcnt(6) lgkmcnt(0)\n\ts_barrier" ::: "memory"); SFENCE; \
        SRD(AY, BY, XY, (t) + 1); SFENCE; \
        SPIECE((t) + 4, 0); SMM(AX, BX, 0); SFENCE; \
        SPIECE((t) + 4, 1); SMM(AX, BX, 1); SFENCE; \
        SPIECE((t) + 4, 2); SPIECE((t) + 4, 3); \
        if (extra) { _Pragma("unroll") for (int k = 0; k < 2; ++k) _Pragma("unroll") for (int m = 0; m < 2; ++m) acc[m][2] = MFMA16(XX[k], AX[m][k], acc[m][2]); } SFENCE; \
    } while (0)
    f32x4 acc[2][3];
#pragma unroll
    for (int m = 0; m < 2; ++m)
#pragma unroll
        for (int n = 0; n < 3; ++n) acc[m][n] = (f32x4){0.f, 0.f, 0.f, 0.f};
    bf16x8 A0[2][2], B0[2][2], X0[2], A1[2][2], B1[2][2], X1[2];
    X0[0] = X0[1] = X1[0] = X1[1] = (bf16x8){0, 0, 0, 0, 0, 0, 0, 0};
#pragma unroll
    for (int i = 0; i < 4; ++i) SPIECE(0, i);
#pragma unroll
    for (int i = 0; i < 4; ++i) SPIECE(1, i);
#pragma unroll
    for (int i = 0; i < 4; ++i) SPIECE(2, i);
#pragma unroll
    for (int i = 0; i < 4; ++i) SPIECE(3, i);
    if (extra) WAIT_V(12); else WAIT_V(9);
    BAR;
    SRD(A0, B0, X0, 0);
    for (int t = 0; t < 16; t += 2) {
        SSTEP(A0, B0, X0, A1, B1, X1, t);
        SSTEP(A1, B1, X1, A0, B0, X0, t + 1);
    }
#undef SPIECE
#undef SFENCE
#undef SRD
#undef SMM
#undef SSTEP
#pragma unroll
    for (int m = 0; m < 2; ++m) {
        const int t = rt * 128 + wr * 32 + m * 16 + fr;
        const int h = ct * 2 + wc;
#pragma unroll
        for (int n = 0; n < 2; ++n) {
            const f32x4 v = acc[m][n] * SCALE_A;
            const u32x2 w = {cvtpk(v[0], v[1]), cvtpk(v[2], v[3])};
            const size_t off = ((size_t)((h * 64 + (t >> 5)) * 2 + n)) * 1024 + (size_t)(((t & 31) + 32 * (fq >> 1)) * 16 + (fq & 1) * 8);
            *(u32x2*)(ws + WS_QA + off) = w;
        }
        if (ct == 0) {
            const f32x4 v = acc[m][2];
            const u32x2 w = {cvtpk(v[0], v[1]), cvtpk(v[2], v[3])};
            const size_t off = ((size_t)((t >> 5) * 2 + wc)) * 1024 + (size_t)(((t & 31) + 32 * (fq >> 1)) * 16 + (fq & 1) * 8);
            *(u32x2*)(ws + WS_KA + off) = w;
        } else if (ct == 1) {
            const f32x4 v = acc[m][2];
            const int pos = (t & ~12) | ((t & 4) << 1) | ((t & 8) >> 1);
            const unsigned w0 = cvtpk(v[0], v[1]), w1 = cvtpk(v[2], v[3]);
            const int d = wc * 16 + 4 * fq;
            char* p = ws + WS_VA + (size_t)(pos >> 4) * 1024 + (size_t)((d + 32 * ((pos >> 3) & 1)) * 16 + (pos & 7) * 2);
            *(u16*)(p) = (u16)w0; *(u16*)(p + 16) = (u16)(w0 >> 16); *(u16*)(p + 32) = (u16)w1; *(u16*)(p + 48) = (u16)(w1 >> 16);
        }
    }
    WAIT_V(0);
}

__global__ __launch_bounds__(512, 2) void k_proj(const char* __restrict__ xA, const char* __restrict__ wqA, const char* __restrict__ wkA, const char* __restrict__ wvA,
                                                 const char* __restrict__ xB, const char* __restrict__ ckv, const char* __restrict__ wqB,
                                                 const char* __restrict__ wuk, const char* __restrict__ wuv, char* __restrict__ ws) {
    extern __shared__ __attribute__((aligned(16))) unsigned char smem[];
    ldsp lds = (ldsp)smem;
    const int b = blockIdx.x, xcd = b & 7, idx = b >> 3;
    if (b == 0 && threadIdx.x < 128) *(unsigned*)(ws + WS_FLAG + threadIdx.x * 128) = 0u;
    STAMP(ws, 0, 0);
    warm(xA + (size_t)b * 16384, 2, ldsaddr_of(lds) + 131072);
    warm(wqA + (size_t)b * 8192, 1, ldsaddr_of(lds) + 131072);
    if (idx < 16) {
        const int rt = 4 * (xcd & 1) + (idx & 3), ct = 4 * (xcd >> 1) + (idx >> 2);
        gemm_big<false>(lds, xB + (size_t)rt * 256 * 2048, 2048u, wqB + (size_t)ct * 512, 8192u, 16, 1, ws + WS_QB, rt * 256, ct * 2, SCALE_B);
    } else if (idx < 24) {
        const int i = idx - 16, rt = 4 * (xcd & 1) + (i & 3), cp = 2 * (xcd >> 1) + (i >> 2);
        gemm_big<false>(lds, ckv + (size_t)rt * 256 * 1024, 1024u, wuk + (size_t)cp * 1024, 8192u, 8, 2, ws + WS_KB, rt * 256, cp * 4, 1.f);
    } else {
        const int i = idx - 24, rt = 4 * (xcd & 1) + (i & 3), cp = 2 * (xcd >> 1) + (i >> 2);
        gemm_big<true>(lds, ckv + (size_t)rt * 256 * 1024, 1024u, wuv + (size_t)cp * 1024, 8192u, 8, 2, ws + WS_VB, rt * 256, cp * 4, 1.f);
    }
    STAMP(ws, 0, 1);
    const int rt = 2 * xcd + (idx >> 4), ct = idx & 15;
    gemm_small(lds, xA, wqA, ct == 0 ? wkA : wvA, rt, ct, ws);
    STAMP(ws, 0, 2);
}

DI float rowmax32(const f32x16& p0, const f32x16& p1) {
    float a = fmaxf(fmaxf(p0[0], p0[1]), p1[0]), b = fmaxf(fmaxf(p0[2], p0[3]), p1[1]); a = fmaxf(fmaxf(a, p1[2]), p1[3]);
#pragma unroll
    for (int r = 4; r < 16; r += 4) { a = fmaxf(fmaxf(a, p0[r]), p0[r + 1]); b = fmaxf(fmaxf(b, p0[r + 2]), p0[r + 3]); a = fmaxf(fmaxf(a, p1[r]), p1[r + 1]); b = fmaxf(fmaxf(b, p1[r + 2]), p1[r + 3]); }
    return fmaxf(a, b);
}
DI float swap32max(float m) { const auto rr = __builtin_amdgcn_permlane32_swap(__float_as_uint(m), __float_as_uint(m), false, false); return fmaxf(__uint_as_float(rr[0]), __uint_as_float(rr[1])); }
DI float swap32sum(float m) { const auto rr = __builtin_amdgcn_permlane32_swap(__float_as_uint(m), __float_as_uint(m), false, false); return __uint_as_float(rr[0]) + __uint_as_float(rr[1]); }
DI bf16x8 packp(const f32x16& p, int b) { const u32x4 w = {cvtpk(p[b], p[b + 1]), cvtpk(p[b + 2], p[b + 3]), cvtpk(p[b + 4], p[b + 5]), cvtpk(p[b + 6], p[b + 7])}; return __builtin_bit_cast(bf16x8, w); }
DI f32x16 splat16(float v) { f32x16 r; _Pragma("unroll") for (int i = 0; i < 16; ++i) r[i] = v; return r; }

DI void attn_b_store(char* ws, const f32x16 (&o)[4], float l, int row, int head, int hh) {
    const float inv = 1.f / swap32sum(l);
    char* orow = ws + WS_CTX + (size_t)row * 8192 + (size_t)head * 256 + hh * 16;
#pragma unroll
    for (int dd = 0; dd < 4; ++dd) {
        unsigned w[8];
#pragma unroll
        for (int i = 0; i < 4; ++i) { w[2 * i] = cvtpk(o[dd][4 * i] * inv, o[dd][4 * i + 1] * inv); w[2 * i + 1] = cvtpk(o[dd][4 * i + 2] * inv, o[dd][4 * i + 3] * inv); }
#pragma unroll
        for (int k = 0; k < 4; k += 2) {
            const auto x0 = __builtin_amdgcn_permlane32_swap(w[2 * k], w[2 * k + 2], false, false);
            const auto x1 = __builtin_amdgcn_permlane32_swap(w[2 * k + 1], w[2 * k + 3], false, false);
            *(u32x4*)(orow + dd * 64 + k * 16) = (u32x4){x0[0], x1[0], x0[1], x1[1]};
        }
    }
}

DI void attn_b(ldsp lds, char* ws, int head, int j) {
    const int tid = threadIdx.x, wq = rfl(tid >> 6), lane = tid & 63, r32 = lane & 31, hh = lane >> 5;
    const char* Kg = ws + WS_KB + (size_t)head * (512u << 10) + (size_t)wq * 4096;
    const char* Vg = ws + WS_VB + (size_t)head * (512u << 10) + (size_t)wq * 4096;
    const char* Qg = ws + WS_QB + (size_t)head * (512u << 10) + lane * 16;
    const unsigned l16 = lane * 16, l0 = ldsaddr_of(lds) + wq * 4096;
    const int NA = 32 - 2 * j;
    ldsp kbase = lds + lane * 16, vbase = lds + 32768 + lane * 16;
#define KT(x) ((x) < NA ? (x) : ((x) > 33 ? 0 : (x) - NA))
#define KPIECE(x, i) glds16(Kg + (size_t)KT(x) * 16384 + (i) * 1024, l16, l0 + ((x) & 1) * 16384 + (i) * 1024)
#define VPIECE(x, i) glds16(Vg + (size_t)KT(x) * 16384 + (i) * 1024, l16, l0 + 32768 + ((x) & 1) * 16384 + (i) * 1024)
#define KFR(x, h, i) ldsr(kbase + ((x) & 1) * 16384 + (h) * 8192 + (i) * 1024)
#define VFR(x, h, i) ldsr(vbase + ((x) & 1) * 16384 + (h) * 8192 + (i) * 1024)
    bf16x8 qf[8], kf[8], vf[8];
#pragma unroll
    for (int i = 0; i < 4; ++i) KPIECE(0, i);
#pragma unroll
    for (int i = 0; i < 4; ++i) VPIECE(0, i);
#pragma unroll
    for (int i = 0; i < 4; ++i) KPIECE(1, i);
#pragma unroll
    for (int s = 0; s < 8; ++s) qf[s] = *(const bf16x8*)(Qg + (size_t)(((15 - j) * 4 + wq) * 8 + s) * 1024);
    asm volatile("" :: "v"(qf[0]), "v"(qf[1]), "v"(qf[2]), "v"(qf[3]), "v"(qf[4]), "v"(qf[5]), "v"(qf[6]), "v"(qf[7]));
    WAIT_V(0); BAR;
    f32x16 o[4]; o[0] = splat16(0.f); o[1] = o[0]; o[2] = o[0]; o[3] = o[0];
    f32x16 sA, sB, negm;
    float mref, l = 0.f;
    {
#pragma unroll
        for (int i = 0; i < 8; ++i) kf[i] = KFR(0, 0, i);
        sA = splat16(0.f);
#pragma unroll
        for (int i = 0; i < 8; ++i) sA = MFMA32(kf[i], qf[i], sA);
#pragma unroll
        for (int i = 0; i < 4; ++i) kf[i] = KFR(0, 1, i);
        float m = fmaxf(sA[0], sA[1]);
#pragma unroll
        for (int r = 2; r < 16; r += 2) m = fmaxf(fmaxf(m, sA[r]), sA[r + 1]);
        mref = swap32max(m);
        sA -= mref; negm = splat16(-mref); asm volatile("" : "+v"(negm));
    }
    u32x4 pw0, pw1;
#define EXPL(A, e) do { A[e] = __builtin_amdgcn_exp2f(A[e]); l += A[e]; } while (0)
#define SM2(A, e, PW, w) do { EXPL(A, e); EXPL(A, (e) + 1); PW[w] = cvtpk(A[e], A[(e) + 1]); asm volatile("" : "+v"(PW), "+v"(l)); } while (0)
#define PK(A, e, PW, w) do { PW[w] = cvtpk(A[e], A[(e) + 1]); asm volatile("" : "+v"(PW), "+v"(l)); } while (0)
#define PINL asm volatile("" : "+v"(l))
#define FENCE do { asm volatile("" ::: "memory"); __builtin_amdgcn_sched_barrier(0); } while (0)
#define PF(k) __builtin_bit_cast(bf16x8, pw##k)
#define MX(a, b) do { ma = fmaxf(fmaxf(ma, a), b); asm volatile("" : "+v"(ma)); } while (0)
#define HSTEP(A, B, u, H, QRL, DEC) do { \
        const int xn = (u) + (H), hn = (H) ^ 1;                \
        kf[4] = KFR(xn, hn, 4); B = MFMA32(kf[0], qf[0], negm); SM2(A, 0, pw0, 0);  FENCE; \
        kf[5] = KFR(xn, hn, 5); B = MFMA32(kf[1], qf[1], B);    EXPL(A, 2); PINL; FENCE; \
        kf[6] = KFR(xn, hn, 6); B = MFMA32(kf[2], qf[2], B);    EXPL(A, 3); PK(A, 2, pw0, 1); FENCE; \
        kf[7] = KFR(xn, hn, 7); B = MFMA32(kf[3], qf[3], B);    EXPL(A, 4); PINL; FENCE; \
        if (H) VPIECE((u) + 1, 0); B = MFMA32(kf[4], qf[4], B); EXPL(A, 5); PK(A, 4, pw0, 2); FENCE; \
        if (H) VPIECE((u) + 1, 1); B = MFMA32(kf[5], qf[5], B); SM2(A, 6, pw0, 3); FENCE; \
        if (!(H)) { asm volatile("s_waitcnt vmcnt(0) lgkmcnt(0)\n\ts_barrier" ::: "memory"); FENCE; } \
        vf[0] = VFR(u, H, 0); vf[1] = VFR(u, H, 1); B = MFMA32(kf[6], qf[6], B); EXPL(A, 8); PINL; FENCE; \
        vf[2] = VFR(u, H, 2); vf[3] = VFR(u, H, 3); B = MFMA32(kf[7], qf[7], B); EXPL(A, 9); PK(A, 8, pw1, 0); FENCE; \
        if (QRL) {      \
            const char* qn = ws + WS_QB + (size_t)head * (512u << 10) + (size_t)(j * 4 + wq) * 8192; \
            asm volatile("global_load_dwordx4 %0, %8, %9\n\tglobal_load_dwordx4 %1, %8, %9 offset:1024\n\tglobal_load_dwordx4 %2, %8, %9 offset:2048\n\tglobal_load_dwordx4 %3, %8, %9 offset:3072\n\t" \
                         "global_load_dwordx4 %4, %8, %10\n\tglobal_load_dwordx4 %5, %8, %10 offset:1024\n\tglobal_load_dwordx4 %6, %8, %10 offset:2048\n\tglobal_load_dwordx4 %7, %8, %10 offset:3072" \
                         : "+v"(qf[0]), "+v"(qf[1]), "+v"(qf[2]), "+v"(qf[3]), "+v"(qf[4]), "+v"(qf[5]), "+v"(qf[6]), "+v"(qf[7]) : "v"(l16), "s"(qn), "s"(qn + 4096) : "memory"); \
        } \
        vf[4] = VFR(u, H, 4); vf[5] = VFR(u, H, 5); o[0] = MFMA32(vf[0], PF(0), o[0]); if (H) VPIECE((u) + 1, 2); else KPIECE((u) + 2, 0); SM2(A, 10, pw1, 1); FENCE; \
        vf[6] = VFR(u, H, 6); vf[7] = VFR(u, H, 7); o[1] = MFMA32(vf[1], PF(0), o[1]); if (H) VPIECE((u) + 1, 3); else KPIECE((u) + 2, 1); SM2(A, 12, pw1, 2); FENCE; \
        o[2] = MFMA32(vf[2], PF(0), o[2]); if (!(H)) KPIECE((u) + 2, 2); EXPL(A, 14); PINL; FENCE; \
        o[3] = MFMA32(vf[3], PF(0), o[3]); if (!(H)) KPIECE((u) + 2, 3); EXPL(A, 15); PK(A, 14, pw1, 3); FENCE; \
        { const int dt = (xn < NA ? NA : 34) - xn;             \
          if (dt <= 2) { \
            const int dq = 32 * wq + r32 - 64 * (2 - dt) - 32 * hn - 4 * hh; \
            _Pragma("unroll") for (int r = 0; r < 16; ++r) { const int c = (r & 3) + 8 * (r >> 2); if (c > dq) B[r] = -INFINITY; } \
        } } \
        float ma = B[0]; \
        kf[0] = KFR((u) + 1, H, 0); o[0] = MFMA32(vf[4], PF(1), o[0]); MX(B[1], B[2]); MX(B[3], B[4]); FENCE; \
        kf[1] = KFR((u) + 1, H, 1); o[1] = MFMA32(vf[5], PF(1), o[1]); MX(B[5], B[6]); MX(B[7], B[8]); FENCE; \
        kf[2] = KFR((u) + 1, H, 2); o[2] = MFMA32(vf[6], PF(1), o[2]); MX(B[9], B[10]); MX(B[11], B[12]); FENCE; \
        kf[3] = KFR((u) + 1, H, 3); o[3] = MFMA32(vf[7], PF(1), o[3]); MX(B[13], B[14]); MX(B[15], B[15]); FENCE; \
        if (QRL) asm volatile("s_waitcnt vmcnt(4)" : "+v"(qf[0]), "+v"(qf[1]), "+v"(qf[2]), "+v"(qf[3]), "+v"(qf[4]), "+v"(qf[5]), "+v"(qf[6]), "+v"(qf[7]) :: "memory"); \
        mlast = ma; \
        if (DEC) { if (__any(ma > THR)) { \
            const float dl = fmaxf(swap32max(ma), 0.f), f = __builtin_amdgcn_exp2f(-dl); \
            mref += dl; l *= f; B -= dl; negm -= dl; \
            _Pragma("unroll") for (int d = 0; d < 4; ++d) o[d] *= f; \
        } } \
    } while (0)
    float mlast;
    int u = 0;
    for (; u < NA - 1; ++u) {
        HSTEP(sA, sB, u, 0, false, true);
        HSTEP(sB, sA, u, 1, false, true);
    }
    HSTEP(sA, sB, u, 0, true, true);
    negm = splat16(0.f); asm volatile("" : "+v"(negm));
    HSTEP(sB, sA, u, 1, false, false);
    attn_b_store(ws, o, l, (15 - j) * 128 + 32 * wq + r32, head, hh);
    o[0] = splat16(0.f); o[1] = o[0]; o[2] = o[0]; o[3] = o[0]; l = 0.f;
    mref = swap32max(mlast);
    sA -= mref; negm = splat16(-mref); asm volatile("" : "+v"(negm));
    for (++u; u < 33; ++u) {
        HSTEP(sA, sB, u, 0, false, true);
        HSTEP(sB, sA, u, 1, false, true);
    }
    HSTEP(sA, sB, 33, 0, false, true);
    {
#pragma unroll
        for (int i = 0; i < 8; ++i) vf[i] = VFR(33, 1, i);
#pragma unroll
        for (int e = 0; e < 8; e += 2) SM2(sB, e, pw0, e >> 1);
#pragma unroll
        for (int e = 8; e < 16; e += 2) SM2(sB, e, pw1, (e - 8) >> 1);
#pragma unroll
        for (int d = 0; d < 4; ++d) o[d] = MFMA32(vf[d], PF(0), o[d]);
#pragma unroll
        for (int d = 0; d < 4; ++d) o[d] = MFMA32(vf[4 + d], PF(1), o[d]);
        attn_b_store(ws, o, l, j * 128 + 32 * wq + r32, head, hh);
        WAIT_V(0);
    }
#undef KT
#undef KPIECE
#undef VPIECE
#undef KFR
#undef VFR
#undef EXPL
#undef SM2
#undef PF
#undef MX
#undef HSTEP
#undef PK
#undef PINL
#undef FENCE
}

DI void attn_a(ldsp lds, char* ws, int qb, int hg) {
    const int tid = threadIdx.x, wid = rfl(tid >> 6), lane = tid & 63, r32 = lane & 31, hh = lane >> 5;
    const char* Kg = ws + WS_KA + (size_t)wid * 2048;
    const char* Vg = ws + WS_VA + (size_t)wid * 2048;
    const unsigned l16 = lane * 16, l0 = ldsaddr_of(lds) + wid * 2048;
    constexpr float SHIFT = 40.f;
#define ASTAGE(c) do { const unsigned _b = l0 + ((c) & 3) * 16384; const size_t _o = (size_t)((c) & 15) * 8192; \
        glds16(Kg + _o, l16, _b); glds16(Kg + _o + 1024, l16, _b + 1024); glds16(Vg + _o, l16, _b + 8192); glds16(Vg + _o + 1024, l16, _b + 8192 + 1024); } while (0)
#define AKF(i, u) ldsr(lds + l16 + (((i) >> 1) & 3) * 16384 + ((i) & 1) * 4096 + (u) * 1024)
#define AVF(i, u) ldsr(lds + l16 + 8192 + (((i) >> 1) & 3) * 16384 + ((i) & 1) * 4096 + (u) * 1024)
#define FENCE do { asm volatile("" ::: "memory"); __builtin_amdgcn_sched_barrier(0); } while (0)
#define EX2(A, e, PW, w) do { A[e] = __builtin_amdgcn_exp2f(A[e]); A[(e) + 1] = __builtin_amdgcn_exp2f(A[(e) + 1]); PW[w] = cvtpk(A[e], A[(e) + 1]); asm volatile("" : "+v"(PW)); } while (0)
#define BC(x) __builtin_bit_cast(bf16x8, x)
#define ASTEP(A0, A1, B0, B1, PA0, PA1, PA2, PA3, PB0, PB1, PB2, PB3, i) do { \
        B0 = MFMA32(kf[0], qf[0], negm);  EX2(A0, 0, PA0, 0);  FENCE; \
        B1 = MFMA32(kf[2], qf[0], negm);  EX2(A0, 2, PA0, 1);  FENCE; \
        B0 = MFMA32(kf[1], qf[1], B0);    EX2(A0, 4, PA0, 2);  EX2(A0, 6, PA0, 3); FENCE; \
        B1 = MFMA32(kf[3], qf[1], B1);    EX2(A0, 8, PA1, 0);  FENCE; \
        kf[0] = AKF((i) + 2, 0); kf[1] = AKF((i) + 2, 1); \
        o = MFMA32(vf[0], BC(PB0), o);    EX2(A0, 10, PA1, 1); FENCE; \
        kf[2] = AKF((i) + 2, 2); kf[3] = AKF((i) + 2, 3); \
        ls = MFMA32(ones, BC(PB0), ls);   EX2(A0, 12, PA1, 2); EX2(A0, 14, PA1, 3); FENCE; \
        o = MFMA32(vf[1], BC(PB1), o);    EX2(A1, 0, PA2, 0);  FENCE; \
        ls = MFMA32(ones, BC(PB1), ls);   EX2(A1, 2, PA2, 1);  FENCE; \
        o = MFMA32(vf[2], BC(PB2), o);    EX2(A1, 4, PA2, 2);  EX2(A1, 6, PA2, 3); FENCE; \
        vf[0] = AVF(i, 0); vf[1] = AVF(i, 1); \
        ls = MFMA32(ones, BC(PB2), ls);   EX2(A1, 8, PA3, 0);  FENCE; \
        o = MFMA32(vf[3], BC(PB3), o);    EX2(A1, 10, PA3, 1); FENCE; \
        vf[2] = AVF(i, 2); vf[3] = AVF(i, 3); \
        ls = MFMA32(ones, BC(PB3), ls);   EX2(A1, 12, PA3, 2); EX2(A1, 14, PA3, 3); FENCE; \
        const u32x4 ov = (PA0 | PA1) | (PA2 | PA3); \
        if (__any(((ov[0] | ov[1] | ov[2] | ov[3]) & 0x40004000u) != 0u)) {         \
            const f32x16 z = splat16(0.f); \
            A0 = MFMA32(AKF(i, 0), qf[0], z); A1 = MFMA32(AKF(i, 2), qf[0], z); A0 = MFMA32(AKF(i, 1), qf[1], A0); A1 = MFMA32(AKF(i, 3), qf[1], A1); \
            const float mn = fmaxf(swap32max(rowmax32(A0, A1)), mref), dl = mn - mref, f = __builtin_amdgcn_exp2f(-dl); \
            mref = mn; o *= f; ls *= f; negm -= dl; B0 -= dl; B1 -= dl; A0 += negm; A1 += negm; \
            EX2(A0, 0, PA0, 0); EX2(A0, 2, PA0, 1); EX2(A0, 4, PA0, 2); EX2(A0, 6, PA0, 3); EX2(A0, 8, PA1, 0); EX2(A0, 10, PA1, 1); EX2(A0, 12, PA1, 2); EX2(A0, 14, PA1, 3); \
            EX2(A1, 0, PA2, 0); EX2(A1, 2, PA2, 1); EX2(A1, 4, PA2, 2); EX2(A1, 6, PA2, 3); EX2(A1, 8, PA3, 0); EX2(A1, 10, PA3, 1); EX2(A1, 12, PA3, 2); EX2(A1, 14, PA3, 3); \
        } \
    } while (0)
    ASTAGE(0); ASTAGE(1);
    const bf16x8 ones = {0x3F80, 0x3F80, 0x3F80, 0x3F80, 0x3F80, 0x3F80, 0x3F80, 0x3F80};
    for (int pass = 0; pass < 2; ++pass) {
        const int head = hg * 8 + pass * 4 + wid;
        bf16x8 qf[2];
#pragma unroll
        for (int s = 0; s < 2; ++s) qf[s] = *(const bf16x8*)(ws + WS_QA + (size_t)((head * 64 + qb) * 2 + s) * 1024 + lane * 16);
        asm volatile("" :: "v"(qf[0]), "v"(qf[1]));
        asm volatile("s_waitcnt vmcnt(0) lgkmcnt(0)\n\ts_barrier" ::: "memory");
        f32x16 o = splat16(0.f), ls = splat16(0.f), negm, sA0, sA1, sB0, sB1;
        bf16x8 kf[4], vf[4];
        u32x4 pa0, pa1, pa2, pa3, pb0, pb1, pb2, pb3;
        float mref;
        {
#pragma unroll
            for (int u = 0; u < 4; ++u) kf[u] = AKF(0, u);
            sA0 = MFMA32(kf[0], qf[0], o); sA1 = MFMA32(kf[2], qf[0], o); sA0 = MFMA32(kf[1], qf[1], sA0); sA1 = MFMA32(kf[3], qf[1], sA1);
#pragma unroll
            for (int u = 0; u < 4; ++u) kf[u] = AKF(1, u);
            mref = swap32max(rowmax32(sA0, sA1));
            negm = splat16(-(mref + SHIFT)); asm volatile("" : "+v"(negm));
            sA0 += negm; sA1 += negm;
            pb0 = (u32x4){0, 0, 0, 0}; pb1 = pb0; pb2 = pb0; pb3 = pb0;
#pragma unroll
            for (int u = 0; u < 4; ++u) vf[u] = __builtin_bit_cast(bf16x8, pb0);
        }
        for (int c = 0; c < 16; ++c) {
            asm volatile("s_waitcnt vmcnt(0) lgkmcnt(0)\n\ts_barrier" ::: "memory");
            ASTAGE(c + 2);
            FENCE;
            ASTEP(sA0, sA1, sB0, sB1, pa0, pa1, pa2, pa3, pb0, pb1, pb2, pb3, 2 * c);
            ASTEP(sB0, sB1, sA0, sA1, pb0, pb1, pb2, pb3, pa0, pa1, pa2, pa3, 2 * c + 1);
        }
        o = MFMA32(vf[0], BC(pb0), o); ls = MFMA32(ones, BC(pb0), ls); o = MFMA32(vf[1], BC(pb1), o); ls = MFMA32(ones, BC(pb1), ls);
        o = MFMA32(vf[2], BC(pb2), o); ls = MFMA32(ones, BC(pb2), ls); o = MFMA32(vf[3], BC(pb3), o); ls = MFMA32(ones, BC(pb3), ls);
        const float inv = 1.f / ls[0];
        unsigned w[8];
#pragma unroll
        for (int i = 0; i < 4; ++i) { w[2 * i] = cvtpk(o[4 * i] * inv, o[4 * i + 1] * inv); w[2 * i + 1] = cvtpk(o[4 * i + 2] * inv, o[4 * i + 3] * inv); }
        char* orow = ws + WS_OA + (size_t)(qb * 32 + r32) * 2048 + (size_t)head * 64 + hh * 16;
#pragma unroll
        for (int k = 0; k < 4; k += 2) {
            const auto x0 = __builtin_amdgcn_permlane32_swap(w[2 * k], w[2 * k + 2], false, false);
            const auto x1 = __builtin_amdgcn_permlane32_swap(w[2 * k + 1], w[2 * k + 3], false, false);
            *(u32x4*)(orow + k * 16) = (u32x4){x0[0], x1[0], x0[1], x1[1]};
        }
    }
    WAIT_V(0);
#undef ASTAGE
#undef AKF
#undef AVF
#undef FENCE
#undef EX2
#undef BC
#undef ASTEP
}

__global__ __launch_bounds__(256, 2) void k_attn(char* __restrict__ ws, const char* __restrict__ woA, const char* __restrict__ woB) {
    extern __shared__ __attribute__((aligned(16))) unsigned char smem[];
    ldsp lds = (ldsp)smem;
    const int b = blockIdx.x & 255, xcd = b & 7, idx = b >> 3;
    if (blockIdx.x < 256) {
        STAMP(ws, 1, 0);
        attn_b(lds, ws, xcd * 4 + (idx >> 3), idx & 7);
        STAMP(ws, 1, 1);
    } else {
        { const int wid = rfl(threadIdx.x >> 6); const unsigned l16 = (threadIdx.x & 63) * 16;
          for (int i = 0; i < 8; ++i) glds16(woB + (size_t)b * 32768 + i * 4096 + wid * 1024, l16, ldsaddr_of(lds) + 65536 + wid * 1024);
          for (int i = 0; i < 2; ++i) glds16(woA + (size_t)b * 8192 + i * 4096 + wid * 1024, l16, ldsaddr_of(lds) + 65536 + wid * 1024); }
        attn_a(lds, ws, b >> 2, b & 3);
    }
}

DI void gemm_mid(ldsp lds, const char* gA, unsigned lda2, const char* gW, int nsteps, f32x4 (&acc)[4][2], char* ws = nullptr) {
    TSDECL;
    const int tid = threadIdx.x, wid = rfl(tid >> 6), lane = tid & 63, wr = wid >> 2, wc = wid & 3, fr = lane & 15, fq = lane >> 4;
    constexpr unsigned ldw2 = 2048;
    unsigned voffA[2], voffW[2];
#pragma unroll
    for (int i = 0; i < 2; ++i) {
        voffA[i] = (unsigned)((tid >> 3) + 64 * i) * lda2 + (unsigned)(((tid & 7) ^ ((tid >> 3) & 7)) << 4);
        const int kg = (tid >> 7) + 4 * i, ng = (tid >> 5) & 3, r = (tid >> 2) & 7, c = (tid & 3) ^ (((tid >> 7) & 1) << 1);
        voffW[i] = (unsigned)(kg * 8 + r) * ldw2 + (unsigned)(ng * 64 + c * 16);
    }
    const int aoff0 = (wr * 64 + fr) * 128 + ((fq ^ (fr & 7)) << 4), aoff1 = aoff0 ^ 64;
    const int boff0 = 16384 + (fq * 4 + wc) * 512 + ((lane >> 2) & 3) * 64 + (((lane & 3) * 8) ^ ((fq & 1) << 5)), boff1 = boff0 ^ 32;
#define MPIECE(t, i) do { const int _t = (t) < nsteps ? (t) : nsteps - 1; const unsigned _b = ldsaddr_of(lds) + ((t) & 3) * 32768 + wid * 1024; \
        if ((i) < 2) glds16(gA + (size_t)_t * 128, voffA[(i) & 1], _b + ((i) & 1) * 8192); \
        else glds16(gW + (size_t)_t * 64 * ldw2, voffW[(i) & 1], _b + 16384 + ((i) & 1) * 8192); } while (0)
#define MFENCE do { asm volatile("" ::: "memory"); __builtin_amdgcn_sched_barrier(0); } while (0)
#define MRDA(AF, t, m) do { ldsp _b = lds + ((t) & 3) * 32768; AF[m][0] = ldsr(_b + aoff0 + (m) * 2048); AF[m][1] = ldsr(_b + aoff1 + (m) * 2048); } while (0)
#define MRDB(BF, t, k) do { ldsp _b = lds + ((t) & 3) * 32768; BF[0][k] = ldstr2(_b + boff0 + (k) * 8192); BF[1][k] = ldstr2(_b + boff1 + (k) * 8192); } while (0)
#define MM4(AF, BF, k, m0) do { acc[m0][0] = MFMA16(BF[0][k], AF[m0][k], acc[m0][0]); acc[m0][1] = MFMA16(BF[1][k], AF[m0][k], acc[m0][1]); \
        acc[(m0) + 1][0] = MFMA16(BF[0][k], AF[(m0) + 1][k], acc[(m0) + 1][0]); acc[(m0) + 1][1] = MFMA16(BF[1][k], AF[(m0) + 1][k], acc[(m0) + 1][1]); } while (0)
#define MSTEP(AX, BX, AY, BY, t) do { \
        TS(0); asm volatile("s_waitcnt vmcnt(8) lgkmcnt(0)" ::: "memory"); TS(1); asm volatile("s_barrier" ::: "memory"); TS(2); MFENCE; \
        MRDB(BY, (t) + 1, 0); MRDA(AY, (t) + 1, 0); MM4(AX, BX, 0, 0); MPIECE((t) + 4, 0); MFENCE; \
        MRDA(AY, (t) + 1, 1); MRDA(AY, (t) + 1, 2); MM4(AX, BX, 0, 2); MPIECE((t) + 4, 1); MFENCE; \
        MRDB(BY, (t) + 1, 1); MRDA(AY, (t) + 1, 3); MM4(AX, BX, 1, 0); MPIECE((t) + 4, 2); MFENCE; \
        MM4(AX, BX, 1, 2); MPIECE((t) + 4, 3); MFENCE; \
    } while (0)
    bf16x8 A0[4][2], B0[2][2], A1[4][2], B1[2][2];
#pragma unroll
    for (int i = 0; i < 4; ++i) MPIECE(0, i);
#pragma unroll
    for (int i = 0; i < 4; ++i) MPIECE(1, i);
#pragma unroll
    for (int i = 0; i < 4; ++i) MPIECE(2, i);
#pragma unroll
    for (int i = 0; i < 4; ++i) MPIECE(3, i);
    WAIT_V(12); BAR;
    MRDB(B0, 0, 0); MRDB(B0, 0, 1); MRDA(A0, 0, 0); MRDA(A0, 0, 1); MRDA(A0, 0, 2); MRDA(A0, 0, 3);
    for (int t = 0; t < nsteps; t += 2) {
        MSTEP(A0, B0, A1, B1, t);
        MSTEP(A1, B1, A0, B0, t + 1);
    }
#undef MPIECE
#undef MFENCE
#undef MRDA
#undef MRDB
#undef MM4
#undef MSTEP
    TS(0);
    if (ws) TSOUT(ws, 2);
    WAIT_V(0); WAIT_L(0); BAR;
}

DI void store_out(char* outp  , const f32x4 (&acc)[4][2]) {
    const int tid = threadIdx.x, wid = rfl(tid >> 6), lane = tid & 63, wr = wid >> 2, wc = wid & 3, fr = lane & 15, fq = lane >> 4;
    const int c8 = (fq >> 1) | ((fq & 1) << 1);
#pragma unroll
    for (int m = 0; m < 4; ++m) {
        const f32x4 v0 = acc[m][0], v1 = acc[m][1];
        const auto r0 = __builtin_amdgcn_permlane16_swap(cvtpk(v0[0], v0[1]), cvtpk(v1[0], v1[1]), false, false);
        const auto r1 = __builtin_amdgcn_permlane16_swap(cvtpk(v0[2], v0[3]), cvtpk(v1[2], v1[3]), false, false);
        *(u32x4*)(outp + (size_t)(wr * 64 + m * 16 + fr) * 4096 + (size_t)(wc * 64 + c8 * 16)) = (u32x4){r0[0], r1[0], r0[1], r1[1]};
    }
}

constexpr int SPLIT0 = 40;
__global__ __launch_bounds__(512, 2) void k_oproj(const char* __restrict__ woA, const char* __restrict__ woB, char* __restrict__ ws, char* __restrict__ out) {
    extern __shared__ __attribute__((aligned(16))) unsigned char smem[];
    ldsp lds = (ldsp)smem;
    const int b = blockIdx.x, xcd = b & 7, idx = b >> 3, role = xcd >> 2;
    const int rt = 4 * (xcd & 3) + (idx & 3), ct = idx >> 2, tile = rt * 8 + ct;
    const int tid = threadIdx.x;
    f32x4 acc[4][2];
#pragma unroll
    for (int m = 0; m < 4; ++m)
#pragma unroll
        for (int n = 0; n < 2; ++n) acc[m][n] = (f32x4){0.f, 0.f, 0.f, 0.f};
    const char* ctx = ws + WS_CTX + (size_t)rt * 128 * 8192;
    unsigned* flag = (unsigned*)(ws + WS_FLAG + tile * 128);
    char* part = ws + WS_PART + (size_t)tile * 65536;
    STAMP(ws, 2, 0);
    if (role == 1) {
        gemm_mid(lds, ctx + SPLIT0 * 128, 8192u, woB + (size_t)SPLIT0 * 64 * 2048 + ct * 256, 64 - SPLIT0, acc, ws);
        const __amdgpu_buffer_rsrc_t rs = __builtin_amdgcn_make_buffer_rsrc(part, 0, 65536, 0x00020000);
#pragma unroll
        for (int m = 0; m < 4; ++m)
#pragma unroll
            for (int n = 0; n < 2; ++n) __builtin_amdgcn_raw_buffer_store_b128(__builtin_bit_cast(u32x4, acc[m][n]), rs, (m * 2 + n) * 8192 + tid * 16, 0, 16);
        WAIT_V(0);
        __syncthreads();
        if (tid == 0) __hip_atomic_store(flag, 1u, __ATOMIC_RELAXED, __HIP_MEMORY_SCOPE_AGENT);
        STAMP(ws, 2, 1);
#pragma unroll
        for (int m = 0; m < 4; ++m)
#pragma unroll
            for (int n = 0; n < 2; ++n) acc[m][n] = (f32x4){0.f, 0.f, 0.f, 0.f};
        gemm_mid(lds, ws + WS_OA + (size_t)rt * 128 * 2048, 2048u, woA + ct * 256, 16, acc);
        store_out(out + (size_t)rt * 128 * 4096 + ct * 256, acc);
        STAMP(ws, 2, 2);
    } else {
        gemm_mid(lds, ctx, 8192u, woB + ct * 256, SPLIT0, acc, ws);
        STAMP(ws, 2, 1);
        if (tid < 64) {
            while (rfl((int)__hip_atomic_load(flag, __ATOMIC_RELAXED, __HIP_MEMORY_SCOPE_AGENT)) != 1) __builtin_amdgcn_s_sleep(2);
            __builtin_amdgcn_fence(__ATOMIC_ACQUIRE, "agent");
            WAIT_V(0);
        }
        __syncthreads();
        const __amdgpu_buffer_rsrc_t rs = __builtin_amdgcn_make_buffer_rsrc(part, 0, 65536, 0x00020000);
#pragma unroll
        for (int m = 0; m < 4; ++m)
#pragma unroll
            for (int n = 0; n < 2; ++n) acc[m][n] += __builtin_bit_cast(f32x4, __builtin_amdgcn_raw_buffer_load_b128(rs, (m * 2 + n) * 8192 + tid * 16, 0, 16));
        store_out(out + (size_t)rt * 128 * 4096 + 2048 + ct * 256, acc);
        STAMP(ws, 2, 2);
    }
}

extern "C" void kernel_launch(void* const* d_in, const int* in_sizes, int n_in, void* d_out, int out_size, void* d_ws, size_t ws_size, hipStream_t stream) {
    (void)in_sizes; (void)n_in; (void)out_size; (void)ws_size;
    constexpr int LDS1 = 131072 + 8192, LDS2 = 65536 + 4096, LDS3 = 131072;
    (void)hipFuncSetAttribute((const void*)k_proj, hipFuncAttributeMaxDynamicSharedMemorySize, LDS1);
    (void)hipFuncSetAttribute((const void*)k_attn, hipFuncAttributeMaxDynamicSharedMemorySize, LDS2);
    (void)hipFuncSetAttribute((const void*)k_oproj, hipFuncAttributeMaxDynamicSharedMemorySize, LDS3);
    char* ws = (char*)d_ws;
    k_proj<<<256, 512, LDS1, stream>>>((const char*)d_in[0], (const char*)d_in[1], (const char*)d_in[2], (const char*)d_in[3], (const char*)d_in[5],
                                       (const char*)d_in[6], (const char*)d_in[7], (const char*)d_in[8], (const char*)d_in[9], ws);
    k_attn<<<512, 256, LDS2, stream>>>(ws, (const char*)d_in[4], (const char*)d_in[10]);
    k_oproj<<<256, 512, LDS3, stream>>>((const char*)d_in[4], (const char*)d_in[10], ws, (char*)d_out);
}
```

```cpp
#include <hip/hip_runtime.h>
#include <stdint.h>

#define LAS __attribute__((address_space(3)))
#define DI __device__ __forceinline__
typedef unsigned short u16;
typedef short bf16x8 __attribute__((ext_vector_type(8)));
typedef short s16x4 __attribute__((ext_vector_type(4)));
typedef float f32x4 __attribute__((ext_vector_type(4)));
typedef float f32x16 __attribute__((ext_vector_type(16)));
typedef unsigned u32x4 __attribute__((ext_vector_type(4)));
typedef unsigned u32x2 __attribute__((ext_vector_type(2)));
typedef LAS unsigned char* ldsp;

constexpr size_t WS_QB = 0;
constexpr size_t WS_KB = WS_QB + (16u << 20);
constexpr size_t WS_VB = WS_KB + (16u << 20);
constexpr size_t WS_QA = WS_VB + (16u << 20);
constexpr size_t WS_KA = WS_QA + (4u << 20);
constexpr size_t WS_VA = WS_KA + (128u << 10);
constexpr size_t WS_CTX = WS_VA + (128u << 10);
constexpr size_t WS_OA = WS_CTX + (16u << 20);
constexpr size_t WS_PART = WS_OA + (4u << 20);
constexpr size_t WS_FLAG = WS_PART + (8u << 20);

constexpr float LOG2E = 1.4426950408889634f;
constexpr float SCALE_A = 0.17677669529663687f * LOG2E;
constexpr float SCALE_B = 0.08838834764831845f * LOG2E;
constexpr float THR = 40.f;

#define WAIT_V(n) asm volatile("s_waitcnt vmcnt(" #n ")" ::: "memory")
#define WAIT_L(n) asm volatile("s_waitcnt lgkmcnt(" #n ")" ::: "memory")
#define BAR __builtin_amdgcn_s_barrier()
#define SCHED __builtin_amdgcn_sched_barrier(0)
#define MFMA16(a, b, c) __builtin_amdgcn_mfma_f32_16x16x32_bf16(a, b, c, 0, 0, 0)
#define MFMA32(a, b, c) __builtin_amdgcn_mfma_f32_32x32x16_bf16(a, b, c, 0, 0, 0)

typedef float f32x2 __attribute__((ext_vector_type(2)));
typedef __bf16 bf16x2_t __attribute__((ext_vector_type(2)));
DI unsigned cvtpk(float lo, float hi) { const f32x2 v = {lo, hi}; return __builtin_bit_cast(unsigned, __builtin_convertvector(v, bf16x2_t)); }
DI void glds16(const char* sbase, unsigned voff, unsigned ldsaddr) {
    asm volatile("s_mov_b32 m0, %2\n\ts_nop 0\n\tglobal_load_lds_dwordx4 %0, %1" :: "v"(voff), "s"(sbase), "s"(ldsaddr) : "memory");
}
template <int OFF> DI void glds16o(const char* sbase, unsigned voff, unsigned ldsaddr) {
    asm volatile("s_mov_b32 m0, %2\n\ts_nop 0\n\tglobal_load_lds_dwordx4 %0, %1 offset:%3" :: "v"(voff), "s"(sbase), "s"(ldsaddr), "i"(OFF) : "memory");
}
DI unsigned ldsaddr_of(ldsp p) { return (unsigned)(size_t)p; }
DI bf16x8 ldsr(ldsp p) { return *(const LAS bf16x8*)p; }
DI s16x4 ldstr(ldsp p) { return __builtin_amdgcn_ds_read_tr16_b64_v4i16((LAS s16x4*)p); }
DI bf16x8 ldstr2(ldsp p) { const s16x4 a = ldstr(p), b = ldstr(p + 256); return (bf16x8){a[0], a[1], a[2], a[3], b[0], b[1], b[2], b[3]}; }
DI int rfl(int x) { return __builtin_amdgcn_readfirstlane(x); }
#define STAMP(ws, kern, i) do {} while (0)
#define TSDECL do {} while (0)
#define TS(i) do {} while (0)
#define TSOUT(ws, slot) do {} while (0)
#define KEEP(...) do {} while (0)

DI void warm(const char* base, int npieces, unsigned dump) {
    const int wid = rfl(threadIdx.x >> 6); const unsigned l16 = (threadIdx.x & 63) * 16;
    for (int i = 0; i < npieces; ++i) glds16(base + (size_t)i * 8192 + wid * 1024, l16, dump + wid * 1024);
}

constexpr int HTB = 16384;
#define SA(b, h) (((b) * 2 + (h)) * HTB)
#define SB(b, h) ((4 + (b) * 2 + (h)) * HTB)

template <bool SWAP>
DI void gemm_big(ldsp lds, const char* gA, unsigned lda2, const char* gW, unsigned ldw2, int nt, int nunits,
                 char* out, int row0, int head0, float scale) {
    const int tid = threadIdx.x, wid = rfl(tid >> 6), lane = tid & 63, wr = wid >> 2, wc = wid & 3, fr = lane & 15, fq = lane >> 4;
    unsigned voffA[2], voffW[2];
#pragma unroll
    for (int i = 0; i < 2; ++i) {
        voffA[i] = (unsigned)((tid >> 3) + 64 * i) * lda2 + (unsigned)(((tid & 7) ^ ((tid >> 3) & 7)) << 4);
        const int kg = (tid >> 7) + 4 * i, ng = (tid >> 5) & 3, r = (tid >> 2) & 7, c = (tid & 3) ^ (((tid >> 7) & 1) << 1);
        voffW[i] = (unsigned)(kg * 8 + r) * ldw2 + (unsigned)(ng * 64 + c * 16);
    }
    const size_t kstepA = 128, kstepW = (size_t)64 * ldw2, hstepA = (size_t)128 * lda2, hstepW = 256;
    const unsigned ldsw = ldsaddr_of(lds) + (unsigned)wid * 1024u;
    const int rowb = wr * 64 + (SWAP ? 16 * (fr >> 3) + (fr & 7) : fr);
    const int aoff0 = rowb * 128 + ((fq ^ (fr & 7)) << 4), aoff1 = aoff0 ^ 64;
    const int boff0 = (fq * 4 + wc) * 512 + ((lane >> 2) & 3) * 64 + (((lane & 3) * 8) ^ ((fq & 1) << 5)), boff1 = boff0 ^ 32;
#define MOFF(m) (SWAP ? ((m) >> 1) * 4096 + ((m) & 1) * 1024 : (m) * 2048)
#define STAGE(bufoff, gbase, voff) do { _Pragma("unroll") for (int _i = 0; _i < 2; ++_i) glds16((gbase), (voff)[_i], (bufoff) + ldsw + _i * 8192); } while (0)
#define LDA(dst, b, h) do { _Pragma("unroll") for (int m = 0; m < 4; ++m) { dst[m][0] = ldsr(lds + SA(b, h) + aoff0 + MOFF(m)); dst[m][1] = ldsr(lds + SA(b, h) + aoff1 + MOFF(m)); } } while (0)
#define LDB(dst, b, h) do { _Pragma("unroll") for (int k = 0; k < 2; ++k) { dst[0][k] = ldstr2(lds + SB(b, h) + boff0 + k * 8192); dst[1][k] = ldstr2(lds + SB(b, h) + boff1 + k * 8192); } } while (0)
#define MMA(ai, bj, At, Bt) do { __builtin_amdgcn_s_setprio(1); _Pragma("unroll") for (int m = 0; m < 4; ++m) _Pragma("unroll") for (int n = 0; n < 2; ++n) _Pragma("unroll") for (int k = 0; k < 2; ++k) \
        acc[ai][bj][m][n] = SWAP ? MFMA16(At[m][k], Bt[n][k], acc[ai][bj][m][n]) : MFMA16(Bt[n][k], At[m][k], acc[ai][bj][m][n]); __builtin_amdgcn_s_setprio(0); } while (0)
    f32x4 acc[2][2][4][2];
#pragma unroll
    for (int a = 0; a < 2; ++a)
#pragma unroll
        for (int b = 0; b < 2; ++b)
#pragma unroll
            for (int m = 0; m < 4; ++m)
#pragma unroll
                for (int n = 0; n < 2; ++n) acc[a][b][m][n] = (f32x4){0.f, 0.f, 0.f, 0.f};
    bf16x8 At[4][2], B0[2][2], B1[2][2];
    const char* cA = gA; const char* cW = gW;
    STAGE(SB(0, 0), cW, voffW); STAGE(SA(0, 0), cA, voffA); STAGE(SB(0, 1), cW + hstepW, voffW); STAGE(SA(0, 1), cA + hstepA, voffA);
    if (wr == 1) BAR;
    WAIT_V(4); BAR;
    STAGE(SB(1, 0), cW + kstepW, voffW); STAGE(SA(1, 0), cA + kstepA, voffA); STAGE(SB(1, 1), cW + hstepW + kstepW, voffW);
    WAIT_V(6); BAR;
    for (int ui = 0;; ++ui) {
        const bool has_next = ui + 1 < nunits;
        const char* nW = has_next ? cW + 512 : cW;
        for (int t = 0; t < nt; t += 2) {
            const bool last = (t == nt - 2);
            const char* a1 = cA + (size_t)(t + 1) * kstepA;
            const char* a2 = last ? cA : cA + (size_t)(t + 2) * kstepA; const char* b2 = last ? nW : cW + (size_t)(t + 2) * kstepW;
            const char* a3 = a2 + kstepA; const char* b3 = b2 + kstepW;
            LDB(B0, 0, 0); SCHED; LDA(At, 0, 0); STAGE(SA(1, 1), a1 + hstepA, voffA);
            WAIT_L(8); BAR; WAIT_L(0); MMA(0, 0, At, B0); BAR; SCHED;
            LDB(B1, 0, 1); STAGE(SB(0, 0), b2, voffW);
            BAR; WAIT_L(0); MMA(0, 1, At, B1); BAR;
            LDA(At, 0, 1); STAGE(SA(0, 0), a2, voffA);
            BAR; WAIT_L(0); MMA(1, 0, At, B0); BAR; SCHED;
            STAGE(SB(0, 1), b2 + hstepW, voffW);
            WAIT_V(6); BAR; MMA(1, 1, At, B1); BAR;
            LDB(B0, 1, 0); SCHED; LDA(At, 1, 0); STAGE(SA(0, 1), a2 + hstepA, voffA);
            WAIT_L(8); BAR; WAIT_L(0); MMA(0, 0, At, B0); BAR; SCHED;
            LDB(B1, 1, 1); STAGE(SB(1, 0), b3, voffW);
            BAR; WAIT_L(0); MMA(0, 1, At, B1); BAR;
            LDA(At, 1, 1); STAGE(SA(1, 0), a3, voffA);
            BAR; WAIT_L(0); MMA(1, 0, At, B0); BAR; SCHED;
            STAGE(SB(1, 1), b3 + hstepW, voffW);
            WAIT_V(6); BAR; MMA(1, 1, At, B1); BAR;
        }
        const int h0 = head0 + 2 * ui;
        if constexpr (!SWAP) {
#pragma unroll
            for (int ai = 0; ai < 2; ++ai)
#pragma unroll
                for (int m = 0; m < 4; ++m) {
                    const int t = row0 + ai * 128 + wr * 64 + m * 16 + fr;
#pragma unroll
                    for (int bj = 0; bj < 2; ++bj) {
                        const f32x4 v0 = acc[ai][bj][m][0] * scale, v1 = acc[ai][bj][m][1] * scale;
                        const auto r0 = __builtin_amdgcn_permlane16_swap(cvtpk(v0[0], v0[1]), cvtpk(v1[0], v1[1]), false, false);
                        const auto r1 = __builtin_amdgcn_permlane16_swap(cvtpk(v0[2], v0[3]), cvtpk(v1[2], v1[3]), false, false);
                        const u32x4 w = {r0[0], r1[0], r0[1], r1[1]};
                        const size_t off = ((size_t)(((h0 + bj) * 64 + (t >> 5)) * 8 + 2 * wc + (fq & 1))) * 1024 + (size_t)(((t & 31) + 32 * (fq >> 1)) * 16);
                        *(u32x4*)(out + off) = w;
                    }
                }
        } else {
#pragma unroll
            for (int ai = 0; ai < 2; ++ai)
#pragma unroll
                for (int mp = 0; mp < 2; ++mp) {
                    const int pos = row0 + ai * 128 + wr * 64 + 32 * mp + 8 * fq;
#pragma unroll
                    for (int bj = 0; bj < 2; ++bj)
#pragma unroll
                        for (int n = 0; n < 2; ++n) {
                            const f32x4 a = acc[ai][bj][2 * mp][n], b = acc[ai][bj][2 * mp + 1][n];
                            const u32x4 w = {cvtpk(a[0], a[1]), cvtpk(a[2], a[3]), cvtpk(b[0], b[1]), cvtpk(b[2], b[3])};
                            const size_t off = ((size_t)(((h0 + bj) * 128 + (pos >> 4)) * 4 + wc)) * 1024 + (size_t)((n * 16 + fr + 32 * (fq & 1)) * 16);
                            *(u32x4*)(out + off) = w;
                        }
                }
        }
        if (!has_next) break;
#pragma unroll
        for (int a = 0; a < 2; ++a)
#pragma unroll
            for (int b = 0; b < 2; ++b)
#pragma unroll
                for (int m = 0; m < 4; ++m)
#pragma unroll
                    for (int n = 0; n < 2; ++n) acc[a][b][m][n] = (f32x4){0.f, 0.f, 0.f, 0.f};
        cW = nW;
    }
    WAIT_V(16);
    if (wr == 0) BAR;
    BAR;
#undef MOFF
#undef STAGE
#undef LDA
#undef LDB
#undef MMA
}

DI void gemm_kv(ldsp lds, const char* xA, const char* wk, const char* wv, int r16, char* ws) {
    const int tid = threadIdx.x, wid = rfl(tid >> 6), lane = tid & 63, fr = lane & 15, fq = lane >> 4;
    const unsigned lb = ldsaddr_of(lds) + wid * 16384;
    const unsigned voff = (unsigned)((lane >> 2) * 64 + (((lane & 3) ^ (((lane >> 5) & 1) << 1)) << 4));
#pragma unroll
    for (int i = 0; i < 8; ++i) { glds16(wk + (size_t)wid * 8192 + i * 1024, voff, lb + i * 1024); glds16(wv + (size_t)wid * 8192 + i * 1024, voff, lb + 8192 + i * 1024); }
    bf16x8 xf[4];
#pragma unroll
    for (int k = 0; k < 4; ++k) xf[k] = *(const bf16x8*)(xA + (size_t)(r16 * 16 + fr) * 2048 + wid * 256 + k * 64 + fq * 16);
    asm volatile("" :: "v"(xf[0]), "v"(xf[1]), "v"(xf[2]), "v"(xf[3]));
    WAIT_V(0);
    ldsp wb = lds + wid * 16384 + fq * 512 + ((lane >> 2) & 3) * 64;
    const int c0 = ((lane & 3) * 8) ^ ((fq & 1) << 5);
    f32x4 acc[4];
#pragma unroll
    for (int n = 0; n < 4; ++n) acc[n] = (f32x4){0.f, 0.f, 0.f, 0.f};
#pragma unroll
    for (int k = 0; k < 4; ++k)
#pragma unroll
        for (int n = 0; n < 4; ++n) acc[n] = MFMA16(ldstr2(wb + (n >> 1) * 8192 + k * 2048 + (c0 ^ ((n & 1) << 5))), xf[k], acc[n]);
    WAIT_L(0); BAR;
#pragma unroll
    for (int n = 0; n < 4; ++n) *(LAS f32x4*)(lds + (n * 8 + wid) * 1024 + lane * 16) = acc[n];
    WAIT_L(0); BAR;
    if (wid < 4) {
        f32x4 v = *(const LAS f32x4*)(lds + (wid * 8) * 1024 + lane * 16);
#pragma unroll
        for (int w = 1; w < 8; ++w) v += *(const LAS f32x4*)(lds + (wid * 8 + w) * 1024 + lane * 16);
        const int t = r16 * 16 + fr;
        const unsigned w0 = cvtpk(v[0], v[1]), w1 = cvtpk(v[2], v[3]);
        if (wid < 2) {
            const size_t off = ((size_t)((t >> 5) * 2 + wid)) * 1024 + (size_t)(((t & 31) + 32 * (fq & 1)) * 16 + (fq >> 1) * 8);
            *(u32x2*)(ws + WS_KA + off) = (u32x2){w0, w1};
        } else {
            const int pos = (t & ~12) | ((t & 4) << 1) | ((t & 8) >> 1);
            const int d = (wid - 2) * 16 + 4 * fq;
            char* p = ws + WS_VA + (size_t)(pos >> 4) * 1024 + (size_t)((d + 32 * ((pos >> 3) & 1)) * 16 + (pos & 7) * 2);
            *(u16*)(p) = (u16)w0; *(u16*)(p + 16) = (u16)(w0 >> 16); *(u16*)(p + 32) = (u16)w1; *(u16*)(p + 48) = (u16)(w1 >> 16);
        }
    }
}

__global__ __launch_bounds__(512, 2) void k_proj(const char* __restrict__ xA, const char* __restrict__ wqA, const char* __restrict__ wkA, const char* __restrict__ wvA,
                                                 const char* __restrict__ xB, const char* __restrict__ ckv, const char* __restrict__ wqB,
                                                 const char* __restrict__ wuk, const char* __restrict__ wuv, char* __restrict__ ws) {
    extern __shared__ __attribute__((aligned(16))) unsigned char smem[];
    ldsp lds = (ldsp)smem;
    const int b = blockIdx.x, xcd = b & 7, idx = b >> 3;
    if (b == 0 && threadIdx.x < 128) *(unsigned*)(ws + WS_FLAG + threadIdx.x * 128) = 0u;
    STAMP(ws, 0, 0);
    warm(xA + (size_t)b * 16384, 2, ldsaddr_of(lds) + 131072);
    warm(wqA + (size_t)b * 8192, 1, ldsaddr_of(lds) + 131072);
    if (idx < 16) warm((idx < 8 ? wkA : wvA) + (size_t)(idx & 7) * 8192, 1, ldsaddr_of(lds) + 131072);
    if (idx < 16) {
        const int rt = 4 * (xcd & 1) + (idx & 3), ct = 4 * (xcd >> 1) + (idx >> 2);
        gemm_big<false>(lds, xB + (size_t)rt * 256 * 2048, 2048u, wqB + (size_t)ct * 512, 8192u, 16, 1, ws + WS_QB, rt * 256, ct * 2, SCALE_B);
        STAMP(ws, 0, 1);
        gemm_kv(lds, xA, wkA, wvA, xcd * 16 + idx, ws);
    } else if (idx < 24) {
        const int i = idx - 16, rt = 4 * (xcd & 1) + (i & 3), cp = 2 * (xcd >> 1) + (i >> 2);
        gemm_big<false>(lds, ckv + (size_t)rt * 256 * 1024, 1024u, wuk + (size_t)cp * 1024, 8192u, 8, 2, ws + WS_KB, rt * 256, cp * 4, 1.f);
    } else {
        const int i = idx - 24, rt = 4 * (xcd & 1) + (i & 3), cp = 2 * (xcd >> 1) + (i >> 2);
        gemm_big<true>(lds, ckv + (size_t)rt * 256 * 1024, 1024u, wuv + (size_t)cp * 1024, 8192u, 8, 2, ws + WS_VB, rt * 256, cp * 4, 1.f);
    }
    STAMP(ws, 0, 2);
}

DI float rowmax32(const f32x16& p0, const f32x16& p1) {
    float a = fmaxf(fmaxf(p0[0], p0[1]), p1[0]), b = fmaxf(fmaxf(p0[2], p0[3]), p1[1]); a = fmaxf(fmaxf(a, p1[2]), p1[3]);
#pragma unroll
    for (int r = 4; r < 16; r += 4) { a = fmaxf(fmaxf(a, p0[r]), p0[r + 1]); b = fmaxf(fmaxf(b, p0[r + 2]), p0[r + 3]); a = fmaxf(fmaxf(a, p1[r]), p1[r + 1]); b = fmaxf(fmaxf(b, p1[r + 2]), p1[r + 3]); }
    return fmaxf(a, b);
}
DI float rowmax16(const f32x16& p) { float a = fmaxf(p[0], p[1]); _Pragma("unroll") for (int r = 2; r < 16; r += 2) a = fmaxf(fmaxf(a, p[r]), p[r + 1]); return a; }
#define APRIO_OFF 6
DI float swap32max(float m) { const auto rr = __builtin_amdgcn_permlane32_swap(__float_as_uint(m), __float_as_uint(m), false, false); return fmaxf(__uint_as_float(rr[0]), __uint_as_float(rr[1])); }
DI float swap32sum(float m) { const auto rr = __builtin_amdgcn_permlane32_swap(__float_as_uint(m), __float_as_uint(m), false, false); return __uint_as_float(rr[0]) + __uint_as_float(rr[1]); }
DI bf16x8 packp(const f32x16& p, int b) { const u32x4 w = {cvtpk(p[b], p[b + 1]), cvtpk(p[b + 2], p[b + 3]), cvtpk(p[b + 4], p[b + 5]), cvtpk(p[b + 6], p[b + 7])}; return __builtin_bit_cast(bf16x8, w); }
DI f32x16 splat16(float v) { f32x16 r; _Pragma("unroll") for (int i = 0; i < 16; ++i) r[i] = v; return r; }

DI void attn_b_store(char* ws, const f32x16 (&o)[4], float l, int row, int head, int hh) {
    const float inv = 1.f / swap32sum(l);
    char* orow = ws + WS_CTX + (size_t)((row >> 7) * 64 + 2 * head) * 16384 + (size_t)(row & 127) * 128;
#pragma unroll
    for (int dd = 0; dd < 4; ++dd) {
        unsigned w[8];
#pragma unroll
        for (int i = 0; i < 4; ++i) { w[2 * i] = cvtpk(o[dd][4 * i] * inv, o[dd][4 * i + 1] * inv); w[2 * i + 1] = cvtpk(o[dd][4 * i + 2] * inv, o[dd][4 * i + 3] * inv); }
#pragma unroll
        for (int k = 0; k < 4; k += 2) {
            const auto x0 = __builtin_amdgcn_permlane32_swap(w[2 * k], w[2 * k + 2], false, false);
            const auto x1 = __builtin_amdgcn_permlane32_swap(w[2 * k + 1], w[2 * k + 3], false, false);
            *(u32x4*)(orow + (dd >> 1) * 16384 + ((((dd & 1) * 4 + k + hh) ^ (row & 7)) << 4)) = (u32x4){x0[0], x1[0], x0[1], x1[1]};
        }
    }
}

DI void attn_b(ldsp lds, char* ws, int head, int j) {
    const int tid = threadIdx.x, wq = rfl(tid >> 6), lane = tid & 63, r32 = lane & 31, hh = lane >> 5;
    const char* Kg = ws + WS_KB + (size_t)head * (512u << 10) + (size_t)wq * 4096;
    const char* Vg = ws + WS_VB + (size_t)head * (512u << 10) + (size_t)wq * 4096;
    const char* Qg = ws + WS_QB + (size_t)head * (512u << 10) + lane * 16;
    const unsigned l16 = lane * 16, l0 = ldsaddr_of(lds) + wq * 4096;
    const int NA = 32 - 2 * j;
    ldsp kbase = lds + lane * 16, vbase = lds + 32768 + lane * 16;
#define KT(x) ((x) < NA ? (x) : ((x) > 33 ? 0 : (x) - NA))
#define KPIECE(x, i) glds16o<(i) * 1024>(Kg + (size_t)KT(x) * 16384, l16, l0 + ((x) & 1) * 16384)
#define VPIECE(x, i) glds16o<(i) * 1024>(Vg + (size_t)KT(x) * 16384, l16, l0 + 32768 + ((x) & 1) * 16384)
#define KFR(x, h, i) ldsr(kbase + ((x) & 1) * 16384 + (h) * 8192 + (i) * 1024)
#define VFR(x, h, i) ldsr(vbase + ((x) & 1) * 16384 + (h) * 8192 + (i) * 1024)
    bf16x8 qf[8], kf[8], vf[8];
    KPIECE(0, 0); KPIECE(0, 1); KPIECE(0, 2); KPIECE(0, 3);
    VPIECE(0, 0); VPIECE(0, 1); VPIECE(0, 2); VPIECE(0, 3);
    KPIECE(1, 0); KPIECE(1, 1); KPIECE(1, 2); KPIECE(1, 3);
#pragma unroll
    for (int s = 0; s < 8; ++s) qf[s] = *(const bf16x8*)(Qg + (size_t)(((15 - j) * 4 + wq) * 8 + s) * 1024);
    asm volatile("" :: "v"(qf[0]), "v"(qf[1]), "v"(qf[2]), "v"(qf[3]), "v"(qf[4]), "v"(qf[5]), "v"(qf[6]), "v"(qf[7]));
    WAIT_V(0); BAR;
    f32x16 o[4]; o[0] = splat16(0.f); o[1] = o[0]; o[2] = o[0]; o[3] = o[0];
    f32x16 sA, sB, negm;
    float mref, l = 0.f;
    {
#pragma unroll
        for (int i = 0; i < 8; ++i) kf[i] = KFR(0, 0, i);
        sA = splat16(0.f);
#pragma unroll
        for (int i = 0; i < 8; ++i) sA = MFMA32(kf[i], qf[i], sA);
#pragma unroll
        for (int i = 0; i < 8; ++i) kf[i] = KFR(0, 1, i);
        float m = fmaxf(sA[0], sA[1]);
#pragma unroll
        for (int r = 2; r < 16; r += 2) m = fmaxf(fmaxf(m, sA[r]), sA[r + 1]);
        mref = swap32max(m);
        sA -= mref; negm = splat16(-mref); asm volatile("" : "+v"(negm));
    }
    u32x4 pw0, pw1;
#define EXPL(A, e) do { A[e] = __builtin_amdgcn_exp2f(A[e]); l += A[e]; } while (0)
#define SM2(A, e, PW, w) do { EXPL(A, e); EXPL(A, (e) + 1); PW[w] = cvtpk(A[e], A[(e) + 1]); asm volatile("" : "+v"(PW), "+v"(l)); } while (0)
#define PK(A, e, PW, w) do { PW[w] = cvtpk(A[e], A[(e) + 1]); asm volatile("" : "+v"(PW), "+v"(l)); } while (0)
#define PINL asm volatile("" : "+v"(l))
#define FENCE do { asm volatile("" ::: "memory"); __builtin_amdgcn_sched_barrier(0); } while (0)
#define PF(k) __builtin_bit_cast(bf16x8, pw##k)
#define MX(a, b, c, d) do { ma = fmaxf(fmaxf(ma, a), b); mb = fmaxf(fmaxf(mb, c), d); asm volatile("" : "+v"(ma), "+v"(mb)); } while (0)
#define EX(A, e) do { A[e] = __builtin_amdgcn_exp2f(A[e]); asm volatile("" : "+v"(A[e])); } while (0)
#define AD(A, e) do { l += A[e]; } while (0)
#define HSTEP(A, B, u, H, QRL, DEC) do { \
        const int xn = (u) + (H), hn = (H) ^ 1;                \
        if (!(H)) { asm volatile("s_waitcnt vmcnt(0) lgkmcnt(0)\n\ts_barrier" ::: "memory"); FENCE; } \
        vf[0] = VFR(u, H, 0); B = MFMA32(kf[0], qf[0], negm); asm volatile("" : "+v"(B)); if (!(H)) VPIECE((u) + 1, 3); EX(A, 0); EX(A, 1); FENCE; \
        vf[1] = VFR(u, H, 1); B = MFMA32(kf[1], qf[1], B); asm volatile("" : "+v"(B));    AD(A, 0); AD(A, 1); PK(A, 0, pw0, 0); EX(A, 2); FENCE; \
        vf[2] = VFR(u, H, 2); B = MFMA32(kf[2], qf[2], B); asm volatile("" : "+v"(B));    if (!(H)) VPIECE((u) + 1, 0); AD(A, 2); EX(A, 3); PINL; FENCE; \
        vf[3] = VFR(u, H, 3); B = MFMA32(kf[3], qf[3], B); asm volatile("" : "+v"(B));    AD(A, 3); PK(A, 2, pw0, 1); EX(A, 4); FENCE; \
        vf[4] = VFR(u, H, 4); B = MFMA32(kf[4], qf[4], B); asm volatile("" : "+v"(B));    if (!(H)) VPIECE((u) + 1, 1); AD(A, 4); EX(A, 5); PINL; FENCE; \
        vf[5] = VFR(u, H, 5); B = MFMA32(kf[5], qf[5], B); asm volatile("" : "+v"(B));    AD(A, 5); PK(A, 4, pw0, 2); EX(A, 6); FENCE; \
        vf[6] = VFR(u, H, 6); B = MFMA32(kf[6], qf[6], B); asm volatile("" : "+v"(B));    if (!(H)) VPIECE((u) + 1, 2); AD(A, 6); EX(A, 7); PINL; FENCE; \
        vf[7] = VFR(u, H, 7); B = MFMA32(kf[7], qf[7], B); asm volatile("" : "+v"(B));    AD(A, 7); PK(A, 6, pw0, 3); EX(A, 8); EX(A, 9); FENCE; \
        if (QRL) {      \
            const char* qn = ws + WS_QB + (size_t)head * (512u << 10) + (size_t)(j * 4 + wq) * 8192; \
            asm volatile("global_load_dwordx4 %0, %8, %9\n\tglobal_load_dwordx4 %1, %8, %9 offset:1024\n\tglobal_load_dwordx4 %2, %8, %9 offset:2048\n\tglobal_load_dwordx4 %3, %8, %9 offset:3072\n\t" \
                         "global_load_dwordx4 %4, %8, %10\n\tglobal_load_dwordx4 %5, %8, %10 offset:1024\n\tglobal_load_dwordx4 %6, %8, %10 offset:2048\n\tglobal_load_dwordx4 %7, %8, %10 offset:3072" \
                         : "+v"(qf[0]), "+v"(qf[1]), "+v"(qf[2]), "+v"(qf[3]), "+v"(qf[4]), "+v"(qf[5]), "+v"(qf[6]), "+v"(qf[7]) : "v"(l16), "s"(qn), "s"(qn + 4096) : "memory"); \
        } \
        kf[0] = KFR((u) + 1, H, 0); kf[1] = KFR((u) + 1, H, 1); o[0] = MFMA32(vf[0], PF(0), o[0]); asm volatile("" : "+v"(o[0])); AD(A, 8); AD(A, 9); PK(A, 8, pw1, 0); EX(A, 10); EX(A, 11); FENCE; \
        kf[2] = KFR((u) + 1, H, 2); kf[3] = KFR((u) + 1, H, 3); o[1] = MFMA32(vf[1], PF(0), o[1]); asm volatile("" : "+v"(o[1])); AD(A, 10); AD(A, 11); PK(A, 10, pw1, 1); EX(A, 12); EX(A, 13); FENCE; \
        kf[4] = KFR((u) + 1, H, 4); kf[5] = KFR((u) + 1, H, 5); o[2] = MFMA32(vf[2], PF(0), o[2]); asm volatile("" : "+v"(o[2])); AD(A, 12); AD(A, 13); PK(A, 12, pw1, 2); EX(A, 14); EX(A, 15); FENCE; \
        kf[6] = KFR((u) + 1, H, 6); kf[7] = KFR((u) + 1, H, 7); o[3] = MFMA32(vf[3], PF(0), o[3]); asm volatile("" : "+v"(o[3])); AD(A, 14); AD(A, 15); PK(A, 14, pw1, 3); FENCE; \
        { const int dt = (xn < NA ? NA : 34) - xn;             \
          if (dt <= 2) { \
            const int dq = 32 * wq + r32 - 64 * (2 - dt) - 32 * hn - 4 * hh; \
            _Pragma("unroll") for (int r = 0; r < 16; ++r) { const int c = (r & 3) + 8 * (r >> 2); if (c > dq) B[r] = -INFINITY; } \
        } } \
        float ma = fmaxf(fmaxf(B[0], B[1]), B[2]), mb = fmaxf(fmaxf(B[3], B[4]), B[5]); \
        o[0] = MFMA32(vf[4], PF(1), o[0]); asm volatile("" : "+v"(o[0])); if (!(H)) KPIECE((u) + 2, 0); MX(B[6], B[7], B[8], B[9]); FENCE; \
        o[1] = MFMA32(vf[5], PF(1), o[1]); asm volatile("" : "+v"(o[1])); if (!(H)) KPIECE((u) + 2, 1); MX(B[10], B[11], B[12], B[13]); FENCE; \
        o[2] = MFMA32(vf[6], PF(1), o[2]); asm volatile("" : "+v"(o[2])); if (!(H)) KPIECE((u) + 2, 2); ma = fmaxf(fmaxf(ma, B[14]), B[15]); ma = fmaxf(ma, mb); asm volatile("" : "+v"(ma)); FENCE; \
        o[3] = MFMA32(vf[7], PF(1), o[3]); asm volatile("" : "+v"(o[3])); if (!(H)) KPIECE((u) + 2, 3); FENCE; \
        if (QRL) asm volatile("s_waitcnt vmcnt(4)" : "+v"(qf[0]), "+v"(qf[1]), "+v"(qf[2]), "+v"(qf[3]), "+v"(qf[4]), "+v"(qf[5]), "+v"(qf[6]), "+v"(qf[7]) :: "memory"); \
        mlast = ma; \
        if (DEC) { if (__any(ma > THR)) { \
            const float dl = fmaxf(swap32max(ma), 0.f), f = __builtin_amdgcn_exp2f(-dl); \
            mref += dl; l *= f; B -= dl; negm -= dl; \
            _Pragma("unroll") for (int d = 0; d < 4; ++d) o[d] *= f; \
        } } \
    } while (0)
    float mlast;
    int u = 0;
    for (; u < NA - 1; ++u) {
        HSTEP(sA, sB, u, 0, false, true);
        HSTEP(sB, sA, u, 1, false, true);
    }
    HSTEP(sA, sB, u, 0, true, true);
    negm = splat16(0.f); asm volatile("" : "+v"(negm));
    HSTEP(sB, sA, u, 1, false, false);
    attn_b_store(ws, o, l, (15 - j) * 128 + 32 * wq + r32, head, hh);
    o[0] = splat16(0.f); o[1] = o[0]; o[2] = o[0]; o[3] = o[0]; l = 0.f;
    mref = swap32max(mlast);
    sA -= mref; negm = splat16(-mref); asm volatile("" : "+v"(negm));
    for (++u; u < 33; ++u) {
        HSTEP(sA, sB, u, 0, false, true);
        HSTEP(sB, sA, u, 1, false, true);
    }
    HSTEP(sA, sB, 33, 0, false, true);
    {
#pragma unroll
        for (int i = 0; i < 8; ++i) vf[i] = VFR(33, 1, i);
#pragma unroll
        for (int e = 0; e < 8; e += 2) SM2(sB, e, pw0, e >> 1);
#pragma unroll
        for (int e = 8; e < 16; e += 2) SM2(sB, e, pw1, (e - 8) >> 1);
#pragma unroll
        for (int d = 0; d < 4; ++d) o[d] = MFMA32(vf[d], PF(0), o[d]);
#pragma unroll
        for (int d = 0; d < 4; ++d) o[d] = MFMA32(vf[4 + d], PF(1), o[d]);
        attn_b_store(ws, o, l, j * 128 + 32 * wq + r32, head, hh);
        WAIT_V(0);
    }
#undef KT
#undef KPIECE
#undef VPIECE
#undef KFR
#undef VFR
#undef EXPL
#undef SM2
#undef PF
#undef MX
#undef HSTEP
#undef EX
#undef AD
#undef PK
#undef PINL
#undef FENCE
}

DI void attn_a(ldsp lds, char* ws, const char* xA, const char* wqA, int rt, int hp) {
    const int tid = threadIdx.x, wid = rfl(tid >> 6), lane = tid & 63, r32 = lane & 31, hh = lane >> 5;
    bf16x8 qall[2][2];
    {
        const char* gA = xA + (size_t)rt * 128 * 2048;
        const char* gW = wqA + (size_t)hp * 128;
        const unsigned vA = (unsigned)(tid >> 3) * 2048u + (unsigned)(((tid & 7) ^ ((tid >> 4) & 7)) << 4);
        const unsigned vW = (unsigned)(wid * 8 + ((lane >> 2) & 7)) * 2048u + (unsigned)((lane >> 5) * 64 + (lane & 3) * 16);
        const unsigned lq = ldsaddr_of(lds) + wid * 1024;
#define QSTAGE(t) do { const int _t = (t) < 16 ? (t) : 15; const unsigned _b = lq + ((t) % 3) * 24576; \
            _Pragma("unroll") for (int _i = 0; _i < 4; ++_i) glds16(gA + _t * 128 + _i * 65536, vA, _b + _i * 4096); \
            glds16(gW + (size_t)_t * 131072, vW, _b + 16384); glds16(gW + (size_t)_t * 131072 + 65536, vW, _b + 16384 + 4096); } while (0)
        const int xrow = wid * 32 + r32, xo = xrow * 128, xs = (xrow >> 1) & 7;
        const int wo = 16384 + hh * 1024 + ((lane >> 2) & 3) * 64 + ((lane >> 4) & 1) * 32 + (lane & 3) * 8;
        f32x16 q0 = splat16(0.f), q1 = q0;
        QSTAGE(0); QSTAGE(1);
        for (int t = 0; t < 16; ++t) {
            asm volatile("s_waitcnt vmcnt(6) lgkmcnt(0)\n\ts_barrier" ::: "memory");
            QSTAGE(t + 2);
            ldsp b = lds + (t % 3) * 24576;
#pragma unroll
            for (int s = 0; s < 4; ++s) {
                const bf16x8 xf = ldsr(b + xo + (((2 * s + hh) ^ xs) << 4));
                q0 = MFMA32(ldstr2(b + wo + s * 2048), xf, q0);
                q1 = MFMA32(ldstr2(b + wo + s * 2048 + 512), xf, q1);
            }
        }
#undef QSTAGE
        q0 *= SCALE_A; q1 *= SCALE_A;
        qall[0][0] = packp(q0, 0); qall[0][1] = packp(q0, 8); qall[1][0] = packp(q1, 0); qall[1][1] = packp(q1, 8);
        asm volatile("s_waitcnt vmcnt(0) lgkmcnt(0)\n\ts_barrier" ::: "memory");
    }
    const char* Kg = ws + WS_KA + (size_t)wid * 2048;
    const char* Vg = ws + WS_VA + (size_t)wid * 2048;
    const unsigned l16 = lane * 16, l0 = ldsaddr_of(lds) + wid * 2048;
    constexpr float SHIFT = 40.f;
#define ASTAGE(c) do { const unsigned _b = l0 + ((c) & 3) * 16384; const size_t _o = (size_t)((c) & 15) * 8192; \
        glds16o<0>(Kg + _o, l16, _b); glds16o<1024>(Kg + _o, l16, _b); glds16o<0>(Vg + _o, l16, _b + 8192); glds16o<1024>(Vg + _o, l16, _b + 8192); } while (0)
#define AKF(i, u) ldsr(lds + l16 + (((i) >> 2) & 3) * 16384 + ((i) & 3) * 2048 + (u) * 1024)
#define AVF(i, u) ldsr(lds + l16 + 8192 + (((i) >> 2) & 3) * 16384 + ((i) & 3) * 2048 + (u) * 1024)
#define FENCE do { asm volatile("" ::: "memory"); __builtin_amdgcn_sched_barrier(0); } while (0)
#define EX2(A, e, PW, w, L) do { A[e] = __builtin_amdgcn_exp2f(A[e]); A[(e) + 1] = __builtin_amdgcn_exp2f(A[(e) + 1]); L += A[e]; L += A[(e) + 1]; PW[w] = cvtpk(A[e], A[(e) + 1]); asm volatile("" : "+v"(PW), "+v"(L)); } while (0)
#define BC(x) __builtin_bit_cast(bf16x8, x)
#define PINV(x) asm volatile("" : "+v"(x))
#define ASTEP(A0, A1, B0, B1, PA0, PA1, PA2, PA3, PB0, PB1, PB2, PB3, i) do { \
        float t0 = 0.f, t1 = 0.f; \
        B0 = MFMA32(kf[0], qall[0][0], ng0); PINV(B0); EX2(A0, 0, PA0, 0, t0);  EX2(A0, 2, PA0, 1, t0);  FENCE; \
        B1 = MFMA32(kf[0], qall[1][0], ng1); PINV(B1); EX2(A0, 4, PA0, 2, t0);  EX2(A0, 6, PA0, 3, t0);  FENCE; \
        B0 = MFMA32(kf[1], qall[0][1], B0);  PINV(B0); EX2(A0, 8, PA1, 0, t0);  EX2(A0, 10, PA1, 1, t0); FENCE; \
        B1 = MFMA32(kf[1], qall[1][1], B1);  PINV(B1); EX2(A0, 12, PA1, 2, t0); EX2(A0, 14, PA1, 3, t0); FENCE; \
        kf[0] = AKF((i) + 2, 0); kf[1] = AKF((i) + 2, 1); \
        o0 = MFMA32(vf[0], BC(PB0), o0);     PINV(o0); EX2(A1, 0, PA2, 0, t1);  EX2(A1, 2, PA2, 1, t1);  FENCE; \
        o1 = MFMA32(vf[0], BC(PB2), o1);     PINV(o1); EX2(A1, 4, PA2, 2, t1);  EX2(A1, 6, PA2, 3, t1);  FENCE; \
        o0 = MFMA32(vf[1], BC(PB1), o0);     PINV(o0); EX2(A1, 8, PA3, 0, t1);  EX2(A1, 10, PA3, 1, t1); FENCE; \
        o1 = MFMA32(vf[1], BC(PB3), o1);     PINV(o1); FENCE; \
        vf[0] = AVF(i, 0); vf[1] = AVF(i, 1);          EX2(A1, 12, PA3, 2, t1); EX2(A1, 14, PA3, 3, t1); FENCE; \
        if (__any(!(fmaxf(t0, t1) < 2.f))) {         \
            const f32x16 z = splat16(0.f); const bf16x8 k0 = AKF(i, 0), k1 = AKF(i, 1); \
            A0 = MFMA32(k0, qall[0][0], z); A1 = MFMA32(k0, qall[1][0], z); A0 = MFMA32(k1, qall[0][1], A0); A1 = MFMA32(k1, qall[1][1], A1); \
            const float mn0 = fmaxf(swap32max(rowmax16(A0)), m0), dl0 = mn0 - m0, f0 = __builtin_amdgcn_exp2f(-dl0); \
            const float mn1 = fmaxf(swap32max(rowmax16(A1)), m1), dl1 = mn1 - m1, f1 = __builtin_amdgcn_exp2f(-dl1); \
            m0 = mn0; o0 *= f0; ls0 *= f0; ng0 -= dl0; B0 -= dl0; A0 += ng0; t0 = 0.f; \
            m1 = mn1; o1 *= f1; ls1 *= f1; ng1 -= dl1; B1 -= dl1; A1 += ng1; t1 = 0.f; \
            EX2(A0, 0, PA0, 0, t0); EX2(A0, 2, PA0, 1, t0); EX2(A0, 4, PA0, 2, t0); EX2(A0, 6, PA0, 3, t0); EX2(A0, 8, PA1, 0, t0); EX2(A0, 10, PA1, 1, t0); EX2(A0, 12, PA1, 2, t0); EX2(A0, 14, PA1, 3, t0); \
            EX2(A1, 0, PA2, 0, t1); EX2(A1, 2, PA2, 1, t1); EX2(A1, 4, PA2, 2, t1); EX2(A1, 6, PA2, 3, t1); EX2(A1, 8, PA3, 0, t1); EX2(A1, 10, PA3, 1, t1); EX2(A1, 12, PA3, 2, t1); EX2(A1, 14, PA3, 3, t1); \
        } \
        ls0 += t0; ls1 += t1; \
    } while (0)
    ASTAGE(0); ASTAGE(1);
    asm volatile("s_waitcnt vmcnt(0) lgkmcnt(0)\n\ts_barrier" ::: "memory");
    f32x16 o0 = splat16(0.f), o1 = o0, ng0, ng1, sA0, sA1, sB0, sB1;
    bf16x8 kf[2], vf[2];
    u32x4 pa0, pa1, pa2, pa3, pb0, pb1, pb2, pb3;
    float m0, m1, ls0 = 0.f, ls1 = 0.f;
    {
        kf[0] = AKF(0, 0); kf[1] = AKF(0, 1);
        sA0 = MFMA32(kf[0], qall[0][0], o0); sA1 = MFMA32(kf[0], qall[1][0], o0); sA0 = MFMA32(kf[1], qall[0][1], sA0); sA1 = MFMA32(kf[1], qall[1][1], sA1);
        kf[0] = AKF(1, 0); kf[1] = AKF(1, 1);
        m0 = swap32max(rowmax16(sA0)); m1 = swap32max(rowmax16(sA1));
        ng0 = splat16(-(m0 + SHIFT)); ng1 = splat16(-(m1 + SHIFT)); asm volatile("" : "+v"(ng0), "+v"(ng1));
        sA0 += ng0; sA1 += ng1;
        pb0 = (u32x4){0, 0, 0, 0}; pb1 = pb0; pb2 = pb0; pb3 = pb0;
        vf[0] = __builtin_bit_cast(bf16x8, pb0); vf[1] = vf[0];
    }
    for (int c = 0; c < 16; ++c) {
        if (c == APRIO_OFF) __builtin_amdgcn_s_setprio(0);
        asm volatile("s_waitcnt vmcnt(0) lgkmcnt(0)\n\ts_barrier" ::: "memory");
        ASTAGE(c + 2);
        FENCE;
        ASTEP(sA0, sA1, sB0, sB1, pa0, pa1, pa2, pa3, pb0, pb1, pb2, pb3, 4 * c);
        ASTEP(sB0, sB1, sA0, sA1, pb0, pb1, pb2, pb3, pa0, pa1, pa2, pa3, 4 * c + 1);
        ASTEP(sA0, sA1, sB0, sB1, pa0, pa1, pa2, pa3, pb0, pb1, pb2, pb3, 4 * c + 2);
        ASTEP(sB0, sB1, sA0, sA1, pb0, pb1, pb2, pb3, pa0, pa1, pa2, pa3, 4 * c + 3);
    }
    o0 = MFMA32(vf[0], BC(pb0), o0); o1 = MFMA32(vf[0], BC(pb2), o1); o0 = MFMA32(vf[1], BC(pb1), o0); o1 = MFMA32(vf[1], BC(pb3), o1);
    const int row = rt * 128 + wid * 32 + r32;
    char* orow = ws + WS_OA + (size_t)((row >> 7) * 16 + hp) * 16384 + (size_t)(row & 127) * 128;
#pragma unroll
    for (int hd = 0; hd < 2; ++hd) {
        const f32x16& o = hd ? o1 : o0;
        const float inv = 1.f / swap32sum(hd ? ls1 : ls0);
        unsigned w[8];
#pragma unroll
        for (int i = 0; i < 4; ++i) { w[2 * i] = cvtpk(o[4 * i] * inv, o[4 * i + 1] * inv); w[2 * i + 1] = cvtpk(o[4 * i + 2] * inv, o[4 * i + 3] * inv); }
#pragma unroll
        for (int k = 0; k < 4; k += 2) {
            const auto x0 = __builtin_amdgcn_permlane32_swap(w[2 * k], w[2 * k + 2], false, false);
            const auto x1 = __builtin_amdgcn_permlane32_swap(w[2 * k + 1], w[2 * k + 3], false, false);
            *(u32x4*)(orow + (((hd * 4 + k + hh) ^ (row & 7)) << 4)) = (u32x4){x0[0], x1[0], x0[1], x1[1]};
        }
    }
    WAIT_V(0);
#undef ASTAGE
#undef AKF
#undef AVF
#undef FENCE
#undef EX2
#undef BC
#undef PINV
#undef ASTEP
}

__global__ __launch_bounds__(256, 2) void k_attn(char* __restrict__ ws, const char* __restrict__ woA, const char* __restrict__ woB, const char* __restrict__ xA, const char* __restrict__ wqA) {
    extern __shared__ __attribute__((aligned(16))) unsigned char smem[];
    ldsp lds = (ldsp)smem;
    const int b = blockIdx.x & 255, xcd = b & 7, idx = b >> 3;
    if (blockIdx.x < 256) {
        STAMP(ws, 1, 0);
        attn_b(lds, ws, xcd * 4 + (idx >> 3), idx & 7);
        STAMP(ws, 1, 1);
    } else {
        { const int wid = rfl(threadIdx.x >> 6); const unsigned l16 = (threadIdx.x & 63) * 16;
          for (int i = 0; i < 8; ++i) glds16(woB + (size_t)b * 32768 + i * 4096 + wid * 1024, l16, ldsaddr_of(lds) + 73728 + wid * 1024);
          for (int i = 0; i < 2; ++i) glds16(woA + (size_t)b * 8192 + i * 4096 + wid * 1024, l16, ldsaddr_of(lds) + 73728 + wid * 1024); }
        __builtin_amdgcn_s_setprio(1);
        attn_a(lds, ws, xA, wqA, 2 * xcd + (idx >> 4), idx & 15);
    }
}

constexpr int SPLIT0 = 42;
DI void store_out(char* outp  , const f32x4 (&acc)[4][2]) {
    const int tid = threadIdx.x, wid = rfl(tid >> 6), lane = tid & 63, wr = wid >> 2, wc = wid & 3, fr = lane & 15, fq = lane >> 4;
    const int c8 = (fq >> 1) | ((fq & 1) << 1);
#pragma unroll
    for (int m = 0; m < 4; ++m) {
        const f32x4 v0 = acc[m][0], v1 = acc[m][1];
        const auto r0 = __builtin_amdgcn_permlane16_swap(cvtpk(v0[0], v0[1]), cvtpk(v1[0], v1[1]), false, false);
        const auto r1 = __builtin_amdgcn_permlane16_swap(cvtpk(v0[2], v0[3]), cvtpk(v1[2], v1[3]), false, false);
        *(u32x4*)(outp + (size_t)(wr * 64 + m * 16 + fr) * 4096 + (size_t)(wc * 64 + c8 * 16)) = (u32x4){r0[0], r1[0], r0[1], r1[1]};
    }
}

template <int ROLE>
DI void oproj(ldsp lds, const char* ctx, const char* woB, const char* oa, const char* woA, char* part, unsigned* flag, char* outp) {
    const int tid = threadIdx.x, wid = rfl(tid >> 6), lane = tid & 63, wr = wid >> 2, wc = wid & 3, fr = lane & 15, fq = lane >> 4;
    constexpr int N1 = ROLE ? 64 - SPLIT0 : SPLIT0, NS = ROLE ? N1 + 16 : N1;
    unsigned voffA[2], voffW[2];
#pragma unroll
    for (int i = 0; i < 2; ++i) {
        voffA[i] = (unsigned)(tid * 16 + i * 8192);
        const int kg = (tid >> 7) + 4 * i, ng = (tid >> 5) & 3, r = (tid >> 2) & 7, c = (tid & 3) ^ (((tid >> 7) & 1) << 1);
        voffW[i] = (unsigned)(kg * 8 + r) * 2048u + (unsigned)(ng * 64 + c * 16);
    }
    const int aoff0 = (wr * 64 + fr) * 128 + ((fq ^ (fr & 7)) << 4), aoff1 = aoff0 ^ 64;
    const int boff0 = 16384 + (fq * 4 + wc) * 512 + ((lane >> 2) & 3) * 64 + (((lane & 3) * 8) ^ ((fq & 1) << 5)), boff1 = boff0 ^ 32;
    const __amdgpu_buffer_rsrc_t rs = __builtin_amdgcn_make_buffer_rsrc(part, 0, 65536, 0x00020000);
#define MPIECE(t, i) do { const int _t = (t) < NS ? (t) : NS - 1; const unsigned _b = ldsaddr_of(lds) + ((t) & 3) * 32768 + wid * 1024; \
        if (!ROLE || _t < N1) { \
            if ((i) < 2) glds16(ctx + (size_t)_t * 16384, voffA[(i) & 1], _b + ((i) & 1) * 8192); \
            else glds16(woB + (size_t)_t * 131072, voffW[(i) & 1], _b + 16384 + ((i) & 1) * 8192); \
        } else { \
            if ((i) < 2) glds16(oa + (size_t)(_t - N1) * 16384, voffA[(i) & 1], _b + ((i) & 1) * 8192); \
            else glds16(woA + (size_t)(_t - N1) * 131072, voffW[(i) & 1], _b + 16384 + ((i) & 1) * 8192); \
        } } while (0)
#define MFENCE do { asm volatile("" ::: "memory"); __builtin_amdgcn_sched_barrier(0); } while (0)
#define MRDA(AF, t, m) do { ldsp _b = lds + ((t) & 3) * 32768; AF[m][0] = ldsr(_b + aoff0 + (m) * 2048); AF[m][1] = ldsr(_b + aoff1 + (m) * 2048); } while (0)
#define MRDB(BF, t, k) do { ldsp _b = lds + ((t) & 3) * 32768; BF[0][k] = ldstr2(_b + boff0 + (k) * 8192); BF[1][k] = ldstr2(_b + boff1 + (k) * 8192); } while (0)
#define MM4(AF, BF, k, m0) do { acc[m0][0] = MFMA16(BF[0][k], AF[m0][k], acc[m0][0]); acc[m0][1] = MFMA16(BF[1][k], AF[m0][k], acc[m0][1]); \
        acc[(m0) + 1][0] = MFMA16(BF[0][k], AF[(m0) + 1][k], acc[(m0) + 1][0]); acc[(m0) + 1][1] = MFMA16(BF[1][k], AF[(m0) + 1][k], acc[(m0) + 1][1]); } while (0)
#define MSTEP(AX, BX, AY, BY, t) do { \
        asm volatile("s_waitcnt vmcnt(8) lgkmcnt(0)\n\ts_barrier" ::: "memory"); MFENCE; \
        MRDB(BY, (t) + 1, 0); MRDA(AY, (t) + 1, 0); MM4(AX, BX, 0, 0); MPIECE((t) + 4, 0); MFENCE; \
        MRDA(AY, (t) + 1, 1); MRDA(AY, (t) + 1, 2); MM4(AX, BX, 0, 2); MPIECE((t) + 4, 1); MFENCE; \
        MRDB(BY, (t) + 1, 1); MRDA(AY, (t) + 1, 3); MM4(AX, BX, 1, 0); MPIECE((t) + 4, 2); MFENCE; \
        MM4(AX, BX, 1, 2); MPIECE((t) + 4, 3); MFENCE; \
    } while (0)
    f32x4 acc[4][2];
#pragma unroll
    for (int m = 0; m < 4; ++m)
#pragma unroll
        for (int n = 0; n < 2; ++n) acc[m][n] = (f32x4){0.f, 0.f, 0.f, 0.f};
    bf16x8 A0[4][2], B0[2][2], A1[4][2], B1[2][2];
#pragma unroll
    for (int i = 0; i < 4; ++i) MPIECE(0, i);
#pragma unroll
    for (int i = 0; i < 4; ++i) MPIECE(1, i);
#pragma unroll
    for (int i = 0; i < 4; ++i) MPIECE(2, i);
#pragma unroll
    for (int i = 0; i < 4; ++i) MPIECE(3, i);
    WAIT_V(12); BAR;
    MRDB(B0, 0, 0); MRDB(B0, 0, 1); MRDA(A0, 0, 0); MRDA(A0, 0, 1); MRDA(A0, 0, 2); MRDA(A0, 0, 3);
    if constexpr (ROLE == 1) {
        for (int t = 0; t < N1; t += 2) { MSTEP(A0, B0, A1, B1, t); MSTEP(A1, B1, A0, B0, t + 1); }
#pragma unroll
        for (int m = 0; m < 4; ++m)
#pragma unroll
            for (int n = 0; n < 2; ++n) { __builtin_amdgcn_raw_buffer_store_b128(__builtin_bit_cast(u32x4, acc[m][n]), rs, (m * 2 + n) * 8192 + tid * 16, 0, 16); acc[m][n] = (f32x4){0.f, 0.f, 0.f, 0.f}; }
        MFENCE;
        for (int t = N1; t < N1 + 4; t += 2) { MSTEP(A0, B0, A1, B1, t); MSTEP(A1, B1, A0, B0, t + 1); }
        if (tid == 0) __hip_atomic_store(flag, 1u, __ATOMIC_RELAXED, __HIP_MEMORY_SCOPE_AGENT);
        MFENCE;
        for (int t = N1 + 4; t < NS; t += 2) { MSTEP(A0, B0, A1, B1, t); MSTEP(A1, B1, A0, B0, t + 1); }
        store_out(outp, acc);
    } else {
        constexpr int TP = SPLIT0 - 6;
        for (int t = 0; t < TP; t += 2) { MSTEP(A0, B0, A1, B1, t); MSTEP(A1, B1, A0, B0, t + 1); }
        if (wid == 0) { while (rfl((int)__hip_atomic_load(flag, __ATOMIC_RELAXED, __HIP_MEMORY_SCOPE_AGENT)) != 1) __builtin_amdgcn_s_sleep(2); }
        MFENCE;
        MSTEP(A0, B0, A1, B1, TP);
        u32x4 pp[4][2];
#pragma unroll
        for (int m = 0; m < 4; ++m)
#pragma unroll
            for (int n = 0; n < 2; ++n) pp[m][n] = __builtin_amdgcn_raw_buffer_load_b128(rs, (m * 2 + n) * 8192 + tid * 16, 0, 16);
        MFENCE;
        MSTEP(A1, B1, A0, B0, TP + 1);
        for (int t = TP + 2; t < NS; t += 2) { MSTEP(A0, B0, A1, B1, t); MSTEP(A1, B1, A0, B0, t + 1); }
#pragma unroll
        for (int m = 0; m < 4; ++m)
#pragma unroll
            for (int n = 0; n < 2; ++n) acc[m][n] += __builtin_bit_cast(f32x4, pp[m][n]);
        store_out(outp, acc);
    }
    WAIT_V(0);
#undef MPIECE
#undef MFENCE
#undef MRDA
#undef MRDB
#undef MM4
#undef MSTEP
}

__global__ __launch_bounds__(512, 2) void k_oproj(const char* __restrict__ woA, const char* __restrict__ woB, char* __restrict__ ws, char* __restrict__ out) {
    extern __shared__ __attribute__((aligned(16))) unsigned char smem[];
    ldsp lds = (ldsp)smem;
    const int b = blockIdx.x, xcd = b & 7, idx = b >> 3, role = xcd >> 2;
    const int rt = 4 * (xcd & 3) + (idx & 3), ct = idx >> 2, tile = rt * 8 + ct;
    const char* ctx = ws + WS_CTX + (size_t)rt * 128 * 8192;
    unsigned* flag = (unsigned*)(ws + WS_FLAG + tile * 128);
    char* part = ws + WS_PART + (size_t)tile * 65536;
    STAMP(ws, 2, 0);
    if (role == 1) oproj<1>(lds, ctx + SPLIT0 * 16384, woB + (size_t)SPLIT0 * 131072 + ct * 256, ws + WS_OA + (size_t)rt * 128 * 2048, woA + ct * 256, part, flag, out + (size_t)rt * 128 * 4096 + ct * 256);
    else oproj<0>(lds, ctx, woB + ct * 256, nullptr, nullptr, part, flag, out + (size_t)rt * 128 * 4096 + 2048 + ct * 256);
    STAMP(ws, 2, 1 + role);
}

extern "C" void kernel_launch(void* const* d_in, const int* in_sizes, int n_in, void* d_out, int out_size, void* d_ws, size_t ws_size, hipStream_t stream) {
    (void)in_sizes; (void)n_in; (void)out_size; (void)ws_size;
    constexpr int LDS1 = 131072 + 8192, LDS2 = 73728 + 4096, LDS3 = 131072;
    (void)hipFuncSetAttribute((const void*)k_proj, hipFuncAttributeMaxDynamicSharedMemorySize, LDS1);
    (void)hipFuncSetAttribute((const void*)k_attn, hipFuncAttributeMaxDynamicSharedMemorySize, LDS2);
    (void)hipFuncSetAttribute((const void*)k_oproj, hipFuncAttributeMaxDynamicSharedMemorySize, LDS3);
    char* ws = (char*)d_ws;
    k_proj<<<256, 512, LDS1, stream>>>((const char*)d_in[0], (const char*)d_in[1], (const char*)d_in[2], (const char*)d_in[3], (const char*)d_in[5],
                                       (const char*)d_in[6], (const char*)d_in[7], (const char*)d_in[8], (const char*)d_in[9], ws);
    k_attn<<<512, 256, LDS2, stream>>>(ws, (const char*)d_in[4], (const char*)d_in[10], (const char*)d_in[0], (const char*)d_in[1]);
    k_oproj<<<256, 512, LDS3, stream>>>((const char*)d_in[4], (const char*)d_in[10], ws, (char*)d_out);
}
```

```cpp
#include <hip/hip_runtime.h>
#include <stdint.h>

#define LAS __attribute__((address_space(3)))
#define DI __device__ __forceinline__
typedef unsigned short u16;
typedef short bf16x8 __attribute__((ext_vector_type(8)));
typedef short s16x4 __attribute__((ext_vector_type(4)));
typedef float f32x4 __attribute__((ext_vector_type(4)));
typedef float f32x16 __attribute__((ext_vector_type(16)));
typedef unsigned u32x4 __attribute__((ext_vector_type(4)));
typedef unsigned u32x2 __attribute__((ext_vector_type(2)));
typedef LAS unsigned char* ldsp;

constexpr size_t WS_QB = 0;
constexpr size_t WS_KB = WS_QB + (16u << 20);
constexpr size_t WS_VB = WS_KB + (16u << 20);
constexpr size_t WS_QA = WS_VB + (16u << 20);
constexpr size_t WS_KA = WS_QA + (4u << 20);
constexpr size_t WS_VA = WS_KA + (128u << 10);
constexpr size_t WS_CTX = WS_VA + (128u << 10);
constexpr size_t WS_OA = WS_CTX + (16u << 20);
constexpr size_t WS_PART = WS_OA + (4u << 20);
constexpr size_t WS_FLAG = WS_PART + (8u << 20);

constexpr float LOG2E = 1.4426950408889634f;
constexpr float SCALE_A = 0.17677669529663687f * LOG2E;
constexpr float SCALE_B = 0.08838834764831845f * LOG2E;
constexpr float THR = 40.f;

#define WAIT_V(n) asm volatile("s_waitcnt vmcnt(" #n ")" ::: "memory")
#define WAIT_L(n) asm volatile("s_waitcnt lgkmcnt(" #n ")" ::: "memory")
#define BAR __builtin_amdgcn_s_barrier()
#define SCHED __builtin_amdgcn_sched_barrier(0)
#define MFMA16(a, b, c) __builtin_amdgcn_mfma_f32_16x16x32_bf16(a, b, c, 0, 0, 0)
#define MFMA32(a, b, c) __builtin_amdgcn_mfma_f32_32x32x16_bf16(a, b, c, 0, 0, 0)

typedef float f32x2 __attribute__((ext_vector_type(2)));
typedef __bf16 bf16x2_t __attribute__((ext_vector_type(2)));
DI unsigned cvtpk(float lo, float hi) { const f32x2 v = {lo, hi}; return __builtin_bit_cast(unsigned, __builtin_convertvector(v, bf16x2_t)); }
DI void glds16(const char* sbase, unsigned voff, unsigned ldsaddr) {
    asm volatile("s_mov_b32 m0, %2\n\ts_nop 0\n\tglobal_load_lds_dwordx4 %0, %1" :: "v"(voff), "s"(sbase), "s"(ldsaddr) : "memory");
}
template <int OFF> DI void glds16o(const char* sbase, unsigned voff, unsigned ldsaddr) {
    asm volatile("s_mov_b32 m0, %2\n\ts_nop 0\n\tglobal_load_lds_dwordx4 %0, %1 offset:%3" :: "v"(voff), "s"(sbase), "s"(ldsaddr), "i"(OFF) : "memory");
}
DI unsigned ldsaddr_of(ldsp p) { return (unsigned)(size_t)p; }
DI bf16x8 ldsr(ldsp p) { return *(const LAS bf16x8*)p; }
DI s16x4 ldstr(ldsp p) { return __builtin_amdgcn_ds_read_tr16_b64_v4i16((LAS s16x4*)p); }
DI bf16x8 ldstr2(ldsp p) { const s16x4 a = ldstr(p), b = ldstr(p + 256); return (bf16x8){a[0], a[1], a[2], a[3], b[0], b[1], b[2], b[3]}; }
DI int rfl(int x) { return __builtin_amdgcn_readfirstlane(x); }
#define STAMP(ws, kern, i) do {} while (0)
#define TSDECL do {} while (0)
#define TS(i) do {} while (0)
#define TSOUT(ws, slot) do {} while (0)
#define KEEP(...) do {} while (0)

DI void warm(const char* base, int npieces, unsigned dump) {
    const int wid = rfl(threadIdx.x >> 6); const unsigned l16 = (threadIdx.x & 63) * 16;
    for (int i = 0; i < npieces; ++i) glds16(base + (size_t)i * 8192 + wid * 1024, l16, dump + wid * 1024);
}

constexpr int HTB = 16384;
#define SA(b, h) (((b) * 2 + (h)) * HTB)
#define SB(b, h) ((4 + (b) * 2 + (h)) * HTB)

template <bool SWAP, bool KVT>
DI void gemm_big(ldsp lds, const char* gA, unsigned lda2, const char* gW, unsigned ldw2, int nt, int nunits,
                 char* out, int row0, int head0, float scale, const char* wk, const char* wv, const char* xkv) {
    const int tid = threadIdx.x, wid = rfl(tid >> 6), lane = tid & 63, wr = wid >> 2, wc = wid & 3, fr = lane & 15, fq = lane >> 4;
    unsigned voffA[2], voffW[2];
#pragma unroll
    for (int i = 0; i < 2; ++i) {
        voffA[i] = (unsigned)((tid >> 3) + 64 * i) * lda2 + (unsigned)(((tid & 7) ^ ((tid >> 3) & 7)) << 4);
        const int kg = (tid >> 7) + 4 * i, ng = (tid >> 5) & 3, r = (tid >> 2) & 7, c = (tid & 3) ^ (((tid >> 7) & 1) << 1);
        voffW[i] = (unsigned)(kg * 8 + r) * ldw2 + (unsigned)(ng * 64 + c * 16);
    }
    const size_t kstepA = 128, kstepW = (size_t)64 * ldw2, hstepA = (size_t)128 * lda2, hstepW = 256;
    const unsigned ldsw = ldsaddr_of(lds) + (unsigned)wid * 1024u;
    const int rowb = wr * 64 + (SWAP ? 16 * (fr >> 3) + (fr & 7) : fr);
    const int aoff0 = rowb * 128 + ((fq ^ (fr & 7)) << 4), aoff1 = aoff0 ^ 64;
    const int boff0 = (fq * 4 + wc) * 512 + ((lane >> 2) & 3) * 64 + (((lane & 3) * 8) ^ ((fq & 1) << 5)), boff1 = boff0 ^ 32;
#define MOFF(m) (SWAP ? ((m) >> 1) * 4096 + ((m) & 1) * 1024 : (m) * 2048)
#define STAGE(bufoff, gbase, voff) do { _Pragma("unroll") for (int _i = 0; _i < 2; ++_i) glds16((gbase), (voff)[_i], (bufoff) + ldsw + _i * 8192); } while (0)
    const unsigned voffkv = (unsigned)((lane >> 2) * 64 + (((lane & 3) ^ (((lane >> 5) & 1) << 1)) << 4));
#define KVSTAGE(bufoff) do { const char* _s = (((bufoff) >> 16) ? wv : wk) + (size_t)wid * 8192 + (((bufoff) >> 14) & 3) * 2048; \
        glds16(_s, voffkv, (bufoff) + ldsw); glds16(_s + 1024, voffkv, (bufoff) + ldsw + 8192); } while (0)
#define STAGE2(KVL, bufoff, gbase, voff) do { if (KVL) KVSTAGE(bufoff); else STAGE(bufoff, gbase, voff); } while (0)
#define LDA(dst, b, h) do { _Pragma("unroll") for (int m = 0; m < 4; ++m) { dst[m][0] = ldsr(lds + SA(b, h) + aoff0 + MOFF(m)); dst[m][1] = ldsr(lds + SA(b, h) + aoff1 + MOFF(m)); } } while (0)
#define LDB(dst, b, h) do { _Pragma("unroll") for (int k = 0; k < 2; ++k) { dst[0][k] = ldstr2(lds + SB(b, h) + boff0 + k * 8192); dst[1][k] = ldstr2(lds + SB(b, h) + boff1 + k * 8192); } } while (0)
#define MMA(ai, bj, At, Bt) do { __builtin_amdgcn_s_setprio(1); _Pragma("unroll") for (int m = 0; m < 4; ++m) _Pragma("unroll") for (int n = 0; n < 2; ++n) _Pragma("unroll") for (int k = 0; k < 2; ++k) \
        acc[ai][bj][m][n] = SWAP ? MFMA16(At[m][k], Bt[n][k], acc[ai][bj][m][n]) : MFMA16(Bt[n][k], At[m][k], acc[ai][bj][m][n]); __builtin_amdgcn_s_setprio(0); } while (0)
    f32x4 acc[2][2][4][2];
#pragma unroll
    for (int a = 0; a < 2; ++a)
#pragma unroll
        for (int b = 0; b < 2; ++b)
#pragma unroll
            for (int m = 0; m < 4; ++m)
#pragma unroll
                for (int n = 0; n < 2; ++n) acc[a][b][m][n] = (f32x4){0.f, 0.f, 0.f, 0.f};
    bf16x8 At[4][2], B0[2][2], B1[2][2];
    const char* cA = gA; const char* cW = gW;
    STAGE(SB(0, 0), cW, voffW); STAGE(SA(0, 0), cA, voffA); STAGE(SB(0, 1), cW + hstepW, voffW); STAGE(SA(0, 1), cA + hstepA, voffA);
    if (wr == 1) BAR;
    WAIT_V(4); BAR;
    STAGE(SB(1, 0), cW + kstepW, voffW); STAGE(SA(1, 0), cA + kstepA, voffA); STAGE(SB(1, 1), cW + hstepW + kstepW, voffW);
    WAIT_V(6); BAR;
    for (int ui = 0;; ++ui) {
        const bool has_next = ui + 1 < nunits;
        const char* nW = has_next ? cW + 512 : cW;
#define KBODY(t, KVL) do { \
            const bool last = (t == nt - 2); \
            const char* a1 = cA + (size_t)(t + 1) * kstepA; \
            const char* a2 = last ? cA : cA + (size_t)(t + 2) * kstepA; const char* b2 = last ? nW : cW + (size_t)(t + 2) * kstepW; \
            const char* a3 = a2 + kstepA; const char* b3 = b2 + kstepW; \
            const bool fresh = (ui > 0 && t == 0); \
            LDB(B0, 0, 0); SCHED; LDA(At, 0, 0); if (!fresh) STAGE(SA(1, 1), a1 + hstepA, voffA); \
            WAIT_L(8); BAR; WAIT_L(0); MMA(0, 0, At, B0); BAR; SCHED; \
            LDB(B1, 0, 1); STAGE2(KVL, SB(0, 0), b2, voffW); \
            BAR; WAIT_L(0); MMA(0, 1, At, B1); BAR; \
            LDA(At, 0, 1); STAGE2(KVL, SA(0, 0), a2, voffA); \
            BAR; WAIT_L(0); MMA(1, 0, At, B0); BAR; SCHED; \
            STAGE2(KVL, SB(0, 1), b2 + hstepW, voffW); \
            if (fresh) WAIT_V(22); else WAIT_V(6); \
            BAR; MMA(1, 1, At, B1); BAR; \
            LDB(B0, 1, 0); SCHED; LDA(At, 1, 0); STAGE2(KVL, SA(0, 1), a2 + hstepA, voffA); \
            WAIT_L(8); BAR; WAIT_L(0); MMA(0, 0, At, B0); BAR; SCHED; \
            LDB(B1, 1, 1); STAGE2(KVL, SB(1, 0), b3, voffW); \
            BAR; WAIT_L(0); MMA(0, 1, At, B1); BAR; \
            LDA(At, 1, 1); STAGE2(KVL, SA(1, 0), a3, voffA); \
            BAR; WAIT_L(0); MMA(1, 0, At, B0); BAR; SCHED; \
            STAGE2(KVL, SB(1, 1), b3 + hstepW, voffW); \
            WAIT_V(6); BAR; MMA(1, 1, At, B1); BAR; \
        } while (0)
        for (int t = 0; t < (KVT ? nt - 2 : nt); t += 2) KBODY(t, false);
        if constexpr (KVT) KBODY(nt - 2, true);
        if (has_next) STAGE(SA(1, 1), cA + kstepA + hstepA, voffA);
        if (KVT) { KVSTAGE(SA(1, 1));
            const unsigned vx = (unsigned)(fr * 2048 + fq * 16);
#pragma unroll
            for (int k = 0; k < 4; ++k) glds16(xkv + wid * 256 + k * 64, vx, ldsaddr_of(lds) + 131072 + wid * 4096 + k * 1024); }
        const int h0 = head0 + 2 * ui;
        if constexpr (!SWAP) {
#pragma unroll
            for (int ai = 0; ai < 2; ++ai)
#pragma unroll
                for (int m = 0; m < 4; ++m) {
                    const int t = row0 + ai * 128 + wr * 64 + m * 16 + fr;
#pragma unroll
                    for (int bj = 0; bj < 2; ++bj) {
                        const f32x4 v0 = acc[ai][bj][m][0] * scale, v1 = acc[ai][bj][m][1] * scale;
                        const auto r0 = __builtin_amdgcn_permlane16_swap(cvtpk(v0[0], v0[1]), cvtpk(v1[0], v1[1]), false, false);
                        const auto r1 = __builtin_amdgcn_permlane16_swap(cvtpk(v0[2], v0[3]), cvtpk(v1[2], v1[3]), false, false);
                        const u32x4 w = {r0[0], r1[0], r0[1], r1[1]};
                        const size_t off = ((size_t)(((h0 + bj) * 64 + (t >> 5)) * 8 + 2 * wc + (fq & 1))) * 1024 + (size_t)(((t & 31) + 32 * (fq >> 1)) * 16);
                        *(u32x4*)(out + off) = w;
                    }
                }
        } else {
#pragma unroll
            for (int ai = 0; ai < 2; ++ai)
#pragma unroll
                for (int mp = 0; mp < 2; ++mp) {
                    const int pos = row0 + ai * 128 + wr * 64 + 32 * mp + 8 * fq;
#pragma unroll
                    for (int bj = 0; bj < 2; ++bj)
#pragma unroll
                        for (int n = 0; n < 2; ++n) {
                            const f32x4 a = acc[ai][bj][2 * mp][n], b = acc[ai][bj][2 * mp + 1][n];
                            const u32x4 w = {cvtpk(a[0], a[1]), cvtpk(a[2], a[3]), cvtpk(b[0], b[1]), cvtpk(b[2], b[3])};
                            const size_t off = ((size_t)(((h0 + bj) * 128 + (pos >> 4)) * 4 + wc)) * 1024 + (size_t)((n * 16 + fr + 32 * (fq & 1)) * 16);
                            *(u32x4*)(out + off) = w;
                        }
                }
        }
        if (!has_next) break;
#pragma unroll
        for (int a = 0; a < 2; ++a)
#pragma unroll
            for (int b = 0; b < 2; ++b)
#pragma unroll
                for (int m = 0; m < 4; ++m)
#pragma unroll
                    for (int n = 0; n < 2; ++n) acc[a][b][m][n] = (f32x4){0.f, 0.f, 0.f, 0.f};
        cW = nW;
    }
    WAIT_V(16);
    if (wr == 0) BAR;
    BAR;
#undef MOFF
#undef KVSTAGE
#undef KBODY
#undef STAGE2
#undef STAGE
#undef LDA
#undef LDB
#undef MMA
}

DI void gemm_kv(ldsp lds, const char* xA, const char* wk, const char* wv, int r16, char* ws) {
    const int tid = threadIdx.x, wid = rfl(tid >> 6), lane = tid & 63, fr = lane & 15, fq = lane >> 4;
    bf16x8 xf[4];
#pragma unroll
    for (int k = 0; k < 4; ++k) xf[k] = ldsr(lds + 131072 + wid * 4096 + k * 1024 + lane * 16);
    ldsp wb = lds + wid * 1024 + (fq >> 1) * 8192 + (fq & 1) * 512 + ((lane >> 2) & 3) * 64;
    const int c0 = ((lane & 3) * 8) ^ ((fq & 1) << 5);
    f32x4 acc[4];
#pragma unroll
    for (int n = 0; n < 4; ++n) acc[n] = (f32x4){0.f, 0.f, 0.f, 0.f};
#pragma unroll
    for (int k = 0; k < 4; ++k)
#pragma unroll
        for (int n = 0; n < 4; ++n) acc[n] = MFMA16(ldstr2(wb + ((n >> 1) * 4 + k) * 16384 + (c0 ^ ((n & 1) << 5))), xf[k], acc[n]);
    WAIT_L(0); BAR;
#pragma unroll
    for (int n = 0; n < 4; ++n) *(LAS f32x4*)(lds + (n * 8 + wid) * 1024 + lane * 16) = acc[n];
    WAIT_L(0); BAR;
    if (wid < 4) {
        f32x4 v = *(const LAS f32x4*)(lds + (wid * 8) * 1024 + lane * 16);
#pragma unroll
        for (int w = 1; w < 8; ++w) v += *(const LAS f32x4*)(lds + (wid * 8 + w) * 1024 + lane * 16);
        const int t = r16 * 16 + fr;
        const unsigned w0 = cvtpk(v[0], v[1]), w1 = cvtpk(v[2], v[3]);
        if (wid < 2) {
            const size_t off = ((size_t)((t >> 5) * 2 + wid)) * 1024 + (size_t)(((t & 31) + 32 * (fq & 1)) * 16 + (fq >> 1) * 8);
            *(u32x2*)(ws + WS_KA + off) = (u32x2){w0, w1};
        } else {
            const int pos = (t & ~12) | ((t & 4) << 1) | ((t & 8) >> 1);
            const int d = (wid - 2) * 16 + 4 * fq;
            char* p = ws + WS_VA + (size_t)(pos >> 4) * 1024 + (size_t)((d + 32 * ((pos >> 3) & 1)) * 16 + (pos & 7) * 2);
            *(u16*)(p) = (u16)w0; *(u16*)(p + 16) = (u16)(w0 >> 16); *(u16*)(p + 32) = (u16)w1; *(u16*)(p + 48) = (u16)(w1 >> 16);
        }
    }
}

__global__ __launch_bounds__(512, 2) void k_proj(const char* __restrict__ xA, const char* __restrict__ wqA, const char* __restrict__ wkA, const char* __restrict__ wvA,
                                                 const char* __restrict__ xB, const char* __restrict__ ckv, const char* __restrict__ wqB,
                                                 const char* __restrict__ wuk, const char* __restrict__ wuv, char* __restrict__ ws) {
    extern __shared__ __attribute__((aligned(16))) unsigned char smem[];
    ldsp lds = (ldsp)smem;
    const int b = blockIdx.x, xcd = b & 7, idx = b >> 3;
    if (b == 0 && threadIdx.x < 128) *(unsigned*)(ws + WS_FLAG + threadIdx.x * 128) = 0u;
    STAMP(ws, 0, 0);
    warm(xA + (size_t)b * 16384, 2, ldsaddr_of(lds) + 131072);
    warm(wqA + (size_t)b * 8192, 1, ldsaddr_of(lds) + 131072);
    if (idx < 16) warm((idx < 8 ? wkA : wvA) + (size_t)(idx & 7) * 8192, 1, ldsaddr_of(lds) + 131072);
    if (idx < 16) {
        const int rt = 4 * (xcd & 1) + (idx & 3), ct = 4 * (xcd >> 1) + (idx >> 2);
        gemm_big<false, true>(lds, xB + (size_t)rt * 256 * 2048, 2048u, wqB + (size_t)ct * 512, 8192u, 16, 1, ws + WS_QB, rt * 256, ct * 2, SCALE_B, wkA, wvA, xA + (size_t)(xcd * 16 + idx) * 16 * 2048);
        STAMP(ws, 0, 1);
        gemm_kv(lds, xA, wkA, wvA, xcd * 16 + idx, ws);
    } else if (idx < 24) {
        const int i = idx - 16, rt = 4 * (xcd & 1) + (i & 3), cp = 2 * (xcd >> 1) + (i >> 2);
        gemm_big<false, false>(lds, ckv + (size_t)rt * 256 * 1024, 1024u, wuk + (size_t)cp * 1024, 8192u, 8, 2, ws + WS_KB, rt * 256, cp * 4, 1.f, nullptr, nullptr, nullptr);
    } else {
        const int i = idx - 24, rt = 4 * (xcd & 1) + (i & 3), cp = 2 * (xcd >> 1) + (i >> 2);
        gemm_big<true, false>(lds, ckv + (size_t)rt * 256 * 1024, 1024u, wuv + (size_t)cp * 1024, 8192u, 8, 2, ws + WS_VB, rt * 256, cp * 4, 1.f, nullptr, nullptr, nullptr);
    }
    STAMP(ws, 0, 2);
}

DI float rowmax32(const f32x16& p0, const f32x16& p1) {
    float a = fmaxf(fmaxf(p0[0], p0[1]), p1[0]), b = fmaxf(fmaxf(p0[2], p0[3]), p1[1]); a = fmaxf(fmaxf(a, p1[2]), p1[3]);
#pragma unroll
    for (int r = 4; r < 16; r += 4) { a = fmaxf(fmaxf(a, p0[r]), p0[r + 1]); b = fmaxf(fmaxf(b, p0[r + 2]), p0[r + 3]); a = fmaxf(fmaxf(a, p1[r]), p1[r + 1]); b = fmaxf(fmaxf(b, p1[r + 2]), p1[r + 3]); }
    return fmaxf(a, b);
}
DI float rowmax16(const f32x16& p) { float a = fmaxf(p[0], p[1]); _Pragma("unroll") for (int r = 2; r < 16; r += 2) a = fmaxf(fmaxf(a, p[r]), p[r + 1]); return a; }
#define APRIO_OFF 4
DI float swap32max(float m) { const auto rr = __builtin_amdgcn_permlane32_swap(__float_as_uint(m), __float_as_uint(m), false, false); return fmaxf(__uint_as_float(rr[0]), __uint_as_float(rr[1])); }
DI float swap32sum(float m) { const auto rr = __builtin_amdgcn_permlane32_swap(__float_as_uint(m), __float_as_uint(m), false, false); return __uint_as_float(rr[0]) + __uint_as_float(rr[1]); }
DI bf16x8 packp(const f32x16& p, int b) { const u32x4 w = {cvtpk(p[b], p[b + 1]), cvtpk(p[b + 2], p[b + 3]), cvtpk(p[b + 4], p[b + 5]), cvtpk(p[b + 6], p[b + 7])}; return __builtin_bit_cast(bf16x8, w); }
DI f32x16 splat16(float v) { f32x16 r; _Pragma("unroll") for (int i = 0; i < 16; ++i) r[i] = v; return r; }

DI void attn_b_store(char* ws, const f32x16 (&o)[4], float l, int row, int head, int hh) {
    const float inv = 1.f / swap32sum(l);
    char* orow = ws + WS_CTX + (size_t)((row >> 7) * 64 + 2 * head) * 16384 + (size_t)(row & 127) * 128;
#pragma unroll
    for (int dd = 0; dd < 4; ++dd) {
        unsigned w[8];
#pragma unroll
        for (int i = 0; i < 4; ++i) { w[2 * i] = cvtpk(o[dd][4 * i] * inv, o[dd][4 * i + 1] * inv); w[2 * i + 1] = cvtpk(o[dd][4 * i + 2] * inv, o[dd][4 * i + 3] * inv); }
#pragma unroll
        for (int k = 0; k < 4; k += 2) {
            const auto x0 = __builtin_amdgcn_permlane32_swap(w[2 * k], w[2 * k + 2], false, false);
            const auto x1 = __builtin_amdgcn_permlane32_swap(w[2 * k + 1], w[2 * k + 3], false, false);
            *(u32x4*)(orow + (dd >> 1) * 16384 + ((((dd & 1) * 4 + k + hh) ^ (row & 7)) << 4)) = (u32x4){x0[0], x1[0], x0[1], x1[1]};
        }
    }
}

DI void attn_b(ldsp lds, char* ws, int head, int j) {
    const int tid = threadIdx.x, wq = rfl(tid >> 6), lane = tid & 63, r32 = lane & 31, hh = lane >> 5;
    const char* Kg = ws + WS_KB + (size_t)head * (512u << 10) + (size_t)wq * 4096;
    const char* Vg = ws + WS_VB + (size_t)head * (512u << 10) + (size_t)wq * 4096;
    const char* Qg = ws + WS_QB + (size_t)head * (512u << 10) + lane * 16;
    const unsigned l16 = lane * 16, l0 = ldsaddr_of(lds) + wq * 4096;
    const int NA = 32 - 2 * j;
    ldsp kbase = lds + lane * 16, vbase = lds + 32768 + lane * 16;
#define KT(x) ((x) < NA ? (x) : ((x) > 33 ? 0 : (x) - NA))
#define KPIECE(x, i) glds16o<(i) * 1024>(Kg + (size_t)KT(x) * 16384, l16, l0 + ((x) & 1) * 16384)
#define VPIECE(x, i) glds16o<(i) * 1024>(Vg + (size_t)KT(x) * 16384, l16, l0 + 32768 + ((x) & 1) * 16384)
#define KFR(x, h, i) ldsr(kbase + ((x) & 1) * 16384 + (h) * 8192 + (i) * 1024)
#define VFR(x, h, i) ldsr(vbase + ((x) & 1) * 16384 + (h) * 8192 + (i) * 1024)
    bf16x8 qf[8], kf[8], vf[8];
    KPIECE(0, 0); KPIECE(0, 1); KPIECE(0, 2); KPIECE(0, 3);
    VPIECE(0, 0); VPIECE(0, 1); VPIECE(0, 2); VPIECE(0, 3);
    KPIECE(1, 0); KPIECE(1, 1); KPIECE(1, 2); KPIECE(1, 3);
#pragma unroll
    for (int s = 0; s < 8; ++s) qf[s] = *(const bf16x8*)(Qg + (size_t)(((15 - j) * 4 + wq) * 8 + s) * 1024);
    asm volatile("" :: "v"(qf[0]), "v"(qf[1]), "v"(qf[2]), "v"(qf[3]), "v"(qf[4]), "v"(qf[5]), "v"(qf[6]), "v"(qf[7]));
    WAIT_V(0); BAR;
    f32x16 o[4]; o[0] = splat16(0.f); o[1] = o[0]; o[2] = o[0]; o[3] = o[0];
    f32x16 sA, sB, negm;
    float mref, l = 0.f;
    {
#pragma unroll
        for (int i = 0; i < 8; ++i) kf[i] = KFR(0, 0, i);
        sA = splat16(0.f);
#pragma unroll
        for (int i = 0; i < 8; ++i) sA = MFMA32(kf[i], qf[i], sA);
#pragma unroll
        for (int i = 0; i < 8; ++i) kf[i] = KFR(0, 1, i);
        float m = fmaxf(sA[0], sA[1]);
#pragma unroll
        for (int r = 2; r < 16; r += 2) m = fmaxf(fmaxf(m, sA[r]), sA[r + 1]);
        mref = swap32max(m);
        sA -= mref; negm = splat16(-mref); asm volatile("" : "+v"(negm));
    }
    u32x4 pw0, pw1;
#define EXPL(A, e) do { A[e] = __builtin_amdgcn_exp2f(A[e]); l += A[e]; } while (0)
#define SM2(A, e, PW, w) do { EXPL(A, e); EXPL(A, (e) + 1); PW[w] = cvtpk(A[e], A[(e) + 1]); asm volatile("" : "+v"(PW), "+v"(l)); } while (0)
#define PK(A, e, PW, w) do { PW[w] = cvtpk(A[e], A[(e) + 1]); asm volatile("" : "+v"(PW), "+v"(l)); } while (0)
#define PINL asm volatile("" : "+v"(l))
#define FENCE do { asm volatile("" ::: "memory"); __builtin_amdgcn_sched_barrier(0); } while (0)
#define PF(k) __builtin_bit_cast(bf16x8, pw##k)
#define MX(a, b, c, d) do { ma = fmaxf(fmaxf(ma, a), b); mb = fmaxf(fmaxf(mb, c), d); asm volatile("" : "+v"(ma), "+v"(mb)); } while (0)
#define EX(A, e) do { A[e] = __builtin_amdgcn_exp2f(A[e]); asm volatile("" : "+v"(A[e])); } while (0)
#define AD(A, e) do { l += A[e]; } while (0)
#define HSTEP(A, B, u, H, QRL, DEC) do { \
        const int xn = (u) + (H), hn = (H) ^ 1;                \
        if (!(H)) { asm volatile("s_waitcnt vmcnt(0) lgkmcnt(0)\n\ts_barrier" ::: "memory"); FENCE; } \
        vf[0] = VFR(u, H, 0); B = MFMA32(kf[0], qf[0], negm); asm volatile("" : "+v"(B)); if (!(H)) VPIECE((u) + 1, 3); EX(A, 0); EX(A, 1); FENCE; \
        vf[1] = VFR(u, H, 1); B = MFMA32(kf[1], qf[1], B); asm volatile("" : "+v"(B));    AD(A, 0); AD(A, 1); PK(A, 0, pw0, 0); EX(A, 2); FENCE; \
        vf[2] = VFR(u, H, 2); B = MFMA32(kf[2], qf[2], B); asm volatile("" : "+v"(B));    if (!(H)) VPIECE((u) + 1, 0); AD(A, 2); EX(A, 3); PINL; FENCE; \
        vf[3] = VFR(u, H, 3); B = MFMA32(kf[3], qf[3], B); asm volatile("" : "+v"(B));    AD(A, 3); PK(A, 2, pw0, 1); EX(A, 4); FENCE; \
        vf[4] = VFR(u, H, 4); B = MFMA32(kf[4], qf[4], B); asm volatile("" : "+v"(B));    if (!(H)) VPIECE((u) + 1, 1); AD(A, 4); EX(A, 5); PINL; FENCE; \
        vf[5] = VFR(u, H, 5); B = MFMA32(kf[5], qf[5], B); asm volatile("" : "+v"(B));    AD(A, 5); PK(A, 4, pw0, 2); EX(A, 6); FENCE; \
        vf[6] = VFR(u, H, 6); B = MFMA32(kf[6], qf[6], B); asm volatile("" : "+v"(B));    if (!(H)) VPIECE((u) + 1, 2); AD(A, 6); EX(A, 7); PINL; FENCE; \
        vf[7] = VFR(u, H, 7); B = MFMA32(kf[7], qf[7], B); asm volatile("" : "+v"(B));    AD(A, 7); PK(A, 6, pw0, 3); EX(A, 8); EX(A, 9); FENCE; \
        if (QRL) {      \
            const char* qn = ws + WS_QB + (size_t)head * (512u << 10) + (size_t)(j * 4 + wq) * 8192; \
            asm volatile("global_load_dwordx4 %0, %8, %9\n\tglobal_load_dwordx4 %1, %8, %9 offset:1024\n\tglobal_load_dwordx4 %2, %8, %9 offset:2048\n\tglobal_load_dwordx4 %3, %8, %9 offset:3072\n\t" \
                         "global_load_dwordx4 %4, %8, %10\n\tglobal_load_dwordx4 %5, %8, %10 offset:1024\n\tglobal_load_dwordx4 %6, %8, %10 offset:2048\n\tglobal_load_dwordx4 %7, %8, %10 offset:3072" \
                         : "+v"(qf[0]), "+v"(qf[1]), "+v"(qf[2]), "+v"(qf[3]), "+v"(qf[4]), "+v"(qf[5]), "+v"(qf[6]), "+v"(qf[7]) : "v"(l16), "s"(qn), "s"(qn + 4096) : "memory"); \
        } \
        kf[0] = KFR((u) + 1, H, 0); kf[1] = KFR((u) + 1, H, 1); o[0] = MFMA32(vf[0], PF(0), o[0]); asm volatile("" : "+v"(o[0])); AD(A, 8); AD(A, 9); PK(A, 8, pw1, 0); EX(A, 10); EX(A, 11); FENCE; \
        kf[2] = KFR((u) + 1, H, 2); kf[3] = KFR((u) + 1, H, 3); o[1] = MFMA32(vf[1], PF(0), o[1]); asm volatile("" : "+v"(o[1])); AD(A, 10); AD(A, 11); PK(A, 10, pw1, 1); EX(A, 12); EX(A, 13); FENCE; \
        kf[4] = KFR((u) + 1, H, 4); kf[5] = KFR((u) + 1, H, 5); o[2] = MFMA32(vf[2], PF(0), o[2]); asm volatile("" : "+v"(o[2])); AD(A, 12); AD(A, 13); PK(A, 12, pw1, 2); EX(A, 14); EX(A, 15); FENCE; \
        kf[6] = KFR((u) + 1, H, 6); kf[7] = KFR((u) + 1, H, 7); o[3] = MFMA32(vf[3], PF(0), o[3]); asm volatile("" : "+v"(o[3])); AD(A, 14); AD(A, 15); PK(A, 14, pw1, 3); FENCE; \
        { const int dt = (xn < NA ? NA : 34) - xn;             \
          if (dt <= 2) { \
            const int dq = 32 * wq + r32 - 64 * (2 - dt) - 32 * hn - 4 * hh; \
            _Pragma("unroll") for (int r = 0; r < 16; ++r) { const int c = (r & 3) + 8 * (r >> 2); if (c > dq) B[r] = -INFINITY; } \
        } } \
        float ma = fmaxf(fmaxf(B[0], B[1]), B[2]), mb = fmaxf(fmaxf(B[3], B[4]), B[5]); \
        o[0] = MFMA32(vf[4], PF(1), o[0]); asm volatile("" : "+v"(o[0])); if (!(H)) KPIECE((u) + 2, 0); MX(B[6], B[7], B[8], B[9]); FENCE; \
        o[1] = MFMA32(vf[5], PF(1), o[1]); asm volatile("" : "+v"(o[1])); if (!(H)) KPIECE((u) + 2, 1); MX(B[10], B[11], B[12], B[13]); FENCE; \
        o[2] = MFMA32(vf[6], PF(1), o[2]); asm volatile("" : "+v"(o[2])); if (!(H)) KPIECE((u) + 2, 2); ma = fmaxf(fmaxf(ma, B[14]), B[15]); ma = fmaxf(ma, mb); asm volatile("" : "+v"(ma)); FENCE; \
        o[3] = MFMA32(vf[7], PF(1), o[3]); asm volatile("" : "+v"(o[3])); if (!(H)) KPIECE((u) + 2, 3); FENCE; \
        if (QRL) asm volatile("s_waitcnt vmcnt(4)" : "+v"(qf[0]), "+v"(qf[1]), "+v"(qf[2]), "+v"(qf[3]), "+v"(qf[4]), "+v"(qf[5]), "+v"(qf[6]), "+v"(qf[7]) :: "memory"); \
        mlast = ma; \
        if (DEC) { if (__any(ma > THR)) { \
            const float dl = fmaxf(swap32max(ma), 0.f), f = __builtin_amdgcn_exp2f(-dl); \
            mref += dl; l *= f; B -= dl; negm -= dl; \
            _Pragma("unroll") for (int d = 0; d < 4; ++d) o[d] *= f; \
        } } \
    } while (0)
    float mlast;
    int u = 0;
    for (; u < NA - 1; ++u) {
        HSTEP(sA, sB, u, 0, false, true);
        HSTEP(sB, sA, u, 1, false, true);
    }
    HSTEP(sA, sB, u, 0, true, true);
    negm = splat16(0.f); asm volatile("" : "+v"(negm));
    HSTEP(sB, sA, u, 1, false, false);
    attn_b_store(ws, o, l, (15 - j) * 128 + 32 * wq + r32, head, hh);
    o[0] = splat16(0.f); o[1] = o[0]; o[2] = o[0]; o[3] = o[0]; l = 0.f;
    mref = swap32max(mlast);
    sA -= mref; negm = splat16(-mref); asm volatile("" : "+v"(negm));
    for (++u; u < 33; ++u) {
        HSTEP(sA, sB, u, 0, false, true);
        HSTEP(sB, sA, u, 1, false, true);
    }
    HSTEP(sA, sB, 33, 0, false, true);
    {
#pragma unroll
        for (int i = 0; i < 8; ++i) vf[i] = VFR(33, 1, i);
#pragma unroll
        for (int e = 0; e < 8; e += 2) SM2(sB, e, pw0, e >> 1);
#pragma unroll
        for (int e = 8; e < 16; e += 2) SM2(sB, e, pw1, (e - 8) >> 1);
#pragma unroll
        for (int d = 0; d < 4; ++d) o[d] = MFMA32(vf[d], PF(0), o[d]);
#pragma unroll
        for (int d = 0; d < 4; ++d) o[d] = MFMA32(vf[4 + d], PF(1), o[d]);
        attn_b_store(ws, o, l, j * 128 + 32 * wq + r32, head, hh);
        WAIT_V(0);
    }
#undef KT
#undef KPIECE
#undef VPIECE
#undef KFR
#undef VFR
#undef EXPL
#undef SM2
#undef PF
#undef MX
#undef HSTEP
#undef EX
#undef AD
#undef PK
#undef PINL
#undef FENCE
}

DI void attn_a(ldsp lds, char* ws, const char* xA, const char* wqA, int rt, int hp) {
    const int tid = threadIdx.x, wid = rfl(tid >> 6), lane = tid & 63, r32 = lane & 31, hh = lane >> 5;
    bf16x8 qall[2][2];
    {
        const char* gA = xA + (size_t)rt * 128 * 2048;
        const char* gW = wqA + (size_t)hp * 128;
        const unsigned vA = (unsigned)(tid >> 3) * 2048u + (unsigned)(((tid & 7) ^ ((tid >> 4) & 7)) << 4);
        const unsigned vW = (unsigned)(wid * 8 + ((lane >> 2) & 7)) * 2048u + (unsigned)((lane >> 5) * 64 + (lane & 3) * 16);
        const unsigned lq = ldsaddr_of(lds) + wid * 1024;
#define QSTAGE(t) do { const int _t = (t) < 16 ? (t) : 15; const unsigned _b = lq + ((t) % 3) * 24576; \
            _Pragma("unroll") for (int _i = 0; _i < 4; ++_i) glds16(gA + _t * 128 + _i * 65536, vA, _b + _i * 4096); \
            glds16(gW + (size_t)_t * 131072, vW, _b + 16384); glds16(gW + (size_t)_t * 131072 + 65536, vW, _b + 16384 + 4096); } while (0)
        const int xrow = wid * 32 + r32, xo = xrow * 128, xs = (xrow >> 1) & 7;
        const int wo = 16384 + hh * 1024 + ((lane >> 2) & 3) * 64 + ((lane >> 4) & 1) * 32 + (lane & 3) * 8;
        f32x16 q0 = splat16(0.f), q1 = q0;
        QSTAGE(0); QSTAGE(1);
        for (int t = 0; t < 16; ++t) {
            asm volatile("s_waitcnt vmcnt(6) lgkmcnt(0)\n\ts_barrier" ::: "memory");
            QSTAGE(t + 2);
            ldsp b = lds + (t % 3) * 24576;
#pragma unroll
            for (int s = 0; s < 4; ++s) {
                const bf16x8 xf = ldsr(b + xo + (((2 * s + hh) ^ xs) << 4));
                q0 = MFMA32(ldstr2(b + wo + s * 2048), xf, q0);
                q1 = MFMA32(ldstr2(b + wo + s * 2048 + 512), xf, q1);
            }
        }
#undef QSTAGE
        q0 *= SCALE_A; q1 *= SCALE_A;
        qall[0][0] = packp(q0, 0); qall[0][1] = packp(q0, 8); qall[1][0] = packp(q1, 0); qall[1][1] = packp(q1, 8);
        asm volatile("s_waitcnt vmcnt(0) lgkmcnt(0)\n\ts_barrier" ::: "memory");
    }
    const char* Kg = ws + WS_KA + (size_t)wid * 2048;
    const char* Vg = ws + WS_VA + (size_t)wid * 2048;
    const unsigned l16 = lane * 16, l0 = ldsaddr_of(lds) + wid * 2048;
    constexpr float SHIFT = 40.f;
#define ASTAGE(c) do { const unsigned _b = l0 + ((c) & 3) * 16384; const size_t _o = (size_t)((c) & 15) * 8192; \
        glds16o<0>(Kg + _o, l16, _b); glds16o<1024>(Kg + _o, l16, _b); glds16o<0>(Vg + _o, l16, _b + 8192); glds16o<1024>(Vg + _o, l16, _b + 8192); } while (0)
#define AKF(i, u) ldsr(lds + l16 + (((i) >> 2) & 3) * 16384 + ((i) & 3) * 2048 + (u) * 1024)
#define AVF(i, u) ldsr(lds + l16 + 8192 + (((i) >> 2) & 3) * 16384 + ((i) & 3) * 2048 + (u) * 1024)
#define FENCE do { asm volatile("" ::: "memory"); __builtin_amdgcn_sched_barrier(0); } while (0)
#define EX2(A, e, PW, w, L) do { A[e] = __builtin_amdgcn_exp2f(A[e]); A[(e) + 1] = __builtin_amdgcn_exp2f(A[(e) + 1]); L += A[e]; L += A[(e) + 1]; PW[w] = cvtpk(A[e], A[(e) + 1]); asm volatile("" : "+v"(PW), "+v"(L)); } while (0)
#define BC(x) __builtin_bit_cast(bf16x8, x)
#define PINV(x) asm volatile("" : "+v"(x))
#define ASTEP(A0, A1, B0, B1, PA0, PA1, PA2, PA3, PB0, PB1, PB2, PB3, i) do { \
        float t0 = 0.f, t1 = 0.f; \
        B0 = MFMA32(kf[0], qall[0][0], ng0); PINV(B0); EX2(A0, 0, PA0, 0, t0);  EX2(A0, 2, PA0, 1, t0);  FENCE; \
        B1 = MFMA32(kf[0], qall[1][0], ng1); PINV(B1); EX2(A0, 4, PA0, 2, t0);  EX2(A0, 6, PA0, 3, t0);  FENCE; \
        B0 = MFMA32(kf[1], qall[0][1], B0);  PINV(B0); EX2(A0, 8, PA1, 0, t0);  EX2(A0, 10, PA1, 1, t0); FENCE; \
        B1 = MFMA32(kf[1], qall[1][1], B1);  PINV(B1); EX2(A0, 12, PA1, 2, t0); EX2(A0, 14, PA1, 3, t0); FENCE; \
        kf[0] = AKF((i) + 2, 0); kf[1] = AKF((i) + 2, 1); \
        o0 = MFMA32(vf[0], BC(PB0), o0);     PINV(o0); EX2(A1, 0, PA2, 0, t1);  EX2(A1, 2, PA2, 1, t1);  FENCE; \
        o1 = MFMA32(vf[0], BC(PB2), o1);     PINV(o1); EX2(A1, 4, PA2, 2, t1);  EX2(A1, 6, PA2, 3, t1);  FENCE; \
        o0 = MFMA32(vf[1], BC(PB1), o0);     PINV(o0); EX2(A1, 8, PA3, 0, t1);  EX2(A1, 10, PA3, 1, t1); FENCE; \
        o1 = MFMA32(vf[1], BC(PB3), o1);     PINV(o1); FENCE; \
        vf[0] = AVF(i, 0); vf[1] = AVF(i, 1);          EX2(A1, 12, PA3, 2, t1); EX2(A1, 14, PA3, 3, t1); FENCE; \
        if (__any(!(fmaxf(t0, t1) < 2.f))) {         \
            const f32x16 z = splat16(0.f); const bf16x8 k0 = AKF(i, 0), k1 = AKF(i, 1); \
            A0 = MFMA32(k0, qall[0][0], z); A1 = MFMA32(k0, qall[1][0], z); A0 = MFMA32(k1, qall[0][1], A0); A1 = MFMA32(k1, qall[1][1], A1); \
            const float mn0 = fmaxf(swap32max(rowmax16(A0)), m0), dl0 = mn0 - m0, f0 = __builtin_amdgcn_exp2f(-dl0); \
            const float mn1 = fmaxf(swap32max(rowmax16(A1)), m1), dl1 = mn1 - m1, f1 = __builtin_amdgcn_exp2f(-dl1); \
            m0 = mn0; o0 *= f0; ls0 *= f0; ng0 -= dl0; B0 -= dl0; A0 += ng0; t0 = 0.f; \
            m1 = mn1; o1 *= f1; ls1 *= f1; ng1 -= dl1; B1 -= dl1; A1 += ng1; t1 = 0.f; \
            EX2(A0, 0, PA0, 0, t0); EX2(A0, 2, PA0, 1, t0); EX2(A0, 4, PA0, 2, t0); EX2(A0, 6, PA0, 3, t0); EX2(A0, 8, PA1, 0, t0); EX2(A0, 10, PA1, 1, t0); EX2(A0, 12, PA1, 2, t0); EX2(A0, 14, PA1, 3, t0); \
            EX2(A1, 0, PA2, 0, t1); EX2(A1, 2, PA2, 1, t1); EX2(A1, 4, PA2, 2, t1); EX2(A1, 6, PA2, 3, t1); EX2(A1, 8, PA3, 0, t1); EX2(A1, 10, PA3, 1, t1); EX2(A1, 12, PA3, 2, t1); EX2(A1, 14, PA3, 3, t1); \
        } \
        ls0 += t0; ls1 += t1; \
    } while (0)
    ASTAGE(0); ASTAGE(1);
    asm volatile("s_waitcnt vmcnt(0) lgkmcnt(0)\n\ts_barrier" ::: "memory");
    f32x16 o0 = splat16(0.f), o1 = o0, ng0, ng1, sA0, sA1, sB0, sB1;
    bf16x8 kf[2], vf[2];
    u32x4 pa0, pa1, pa2, pa3, pb0, pb1, pb2, pb3;
    float m0, m1, ls0 = 0.f, ls1 = 0.f;
    {
        kf[0] = AKF(0, 0); kf[1] = AKF(0, 1);
        sA0 = MFMA32(kf[0], qall[0][0], o0); sA1 = MFMA32(kf[0], qall[1][0], o0); sA0 = MFMA32(kf[1], qall[0][1], sA0); sA1 = MFMA32(kf[1], qall[1][1], sA1);
        kf[0] = AKF(1, 0); kf[1] = AKF(1, 1);
        m0 = swap32max(rowmax16(sA0)); m1 = swap32max(rowmax16(sA1));
        ng0 = splat16(-(m0 + SHIFT)); ng1 = splat16(-(m1 + SHIFT)); asm volatile("" : "+v"(ng0), "+v"(ng1));
        sA0 += ng0; sA1 += ng1;
        pb0 = (u32x4){0, 0, 0, 0}; pb1 = pb0; pb2 = pb0; pb3 = pb0;
        vf[0] = __builtin_bit_cast(bf16x8, pb0); vf[1] = vf[0];
    }
    for (int c = 0; c < 16; ++c) {
        if (c == APRIO_OFF) __builtin_amdgcn_s_setprio(0);
        asm volatile("s_waitcnt vmcnt(0) lgkmcnt(0)\n\ts_barrier" ::: "memory");
        ASTAGE(c + 2);
        FENCE;
        ASTEP(sA0, sA1, sB0, sB1, pa0, pa1, pa2, pa3, pb0, pb1, pb2, pb3, 4 * c);
        ASTEP(sB0, sB1, sA0, sA1, pb0, pb1, pb2, pb3, pa0, pa1, pa2, pa3, 4 * c + 1);
        ASTEP(sA0, sA1, sB0, sB1, pa0, pa1, pa2, pa3, pb0, pb1, pb2, pb3, 4 * c + 2);
        ASTEP(sB0, sB1, sA0, sA1, pb0, pb1, pb2, pb3, pa0, pa1, pa2, pa3, 4 * c + 3);
    }
    o0 = MFMA32(vf[0], BC(pb0), o0); o1 = MFMA32(vf[0], BC(pb2), o1); o0 = MFMA32(vf[1], BC(pb1), o0); o1 = MFMA32(vf[1], BC(pb3), o1);
    const int row = rt * 128 + wid * 32 + r32;
    char* orow = ws + WS_OA + (size_t)((row >> 7) * 16 + hp) * 16384 + (size_t)(row & 127) * 128;
#pragma unroll
    for (int hd = 0; hd < 2; ++hd) {
        const f32x16& o = hd ? o1 : o0;
        const float inv = 1.f / swap32sum(hd ? ls1 : ls0);
        unsigned w[8];
#pragma unroll
        for (int i = 0; i < 4; ++i) { w[2 * i] = cvtpk(o[4 * i] * inv, o[4 * i + 1] * inv); w[2 * i + 1] = cvtpk(o[4 * i + 2] * inv, o[4 * i + 3] * inv); }
#pragma unroll
        for (int k = 0; k < 4; k += 2) {
            const auto x0 = __builtin_amdgcn_permlane32_swap(w[2 * k], w[2 * k + 2], false, false);
            const auto x1 = __builtin_amdgcn_permlane32_swap(w[2 * k + 1], w[2 * k + 3], false, false);
            *(u32x4*)(orow + (((hd * 4 + k + hh) ^ (row & 7)) << 4)) = (u32x4){x0[0], x1[0], x0[1], x1[1]};
        }
    }
    WAIT_V(0);
#undef ASTAGE
#undef AKF
#undef AVF
#undef FENCE
#undef EX2
#undef BC
#undef PINV
#undef ASTEP
}

__global__ __launch_bounds__(256, 2) void k_attn(char* __restrict__ ws, const char* __restrict__ woA, const char* __restrict__ woB, const char* __restrict__ xA, const char* __restrict__ wqA) {
    extern __shared__ __attribute__((aligned(16))) unsigned char smem[];
    ldsp lds = (ldsp)smem;
    const int b = blockIdx.x & 255, xcd = b & 7, idx = b >> 3;
    if (blockIdx.x < 256) {
        STAMP(ws, 1, 0);
        attn_b(lds, ws, xcd * 4 + (idx >> 3), idx & 7);
        STAMP(ws, 1, 1);
    } else {
        { const int wid = rfl(threadIdx.x >> 6); const unsigned l16 = (threadIdx.x & 63) * 16;
          for (int i = 0; i < 8; ++i) glds16(woB + (size_t)b * 32768 + i * 4096 + wid * 1024, l16, ldsaddr_of(lds) + 73728 + wid * 1024);
          for (int i = 0; i < 2; ++i) glds16(woA + (size_t)b * 8192 + i * 4096 + wid * 1024, l16, ldsaddr_of(lds) + 73728 + wid * 1024); }
        __builtin_amdgcn_s_setprio(1);
        attn_a(lds, ws, xA, wqA, 2 * xcd + (idx >> 4), idx & 15);
    }
}

constexpr int SPLIT0 = 42;
DI void store_out(char* outp  , const f32x4 (&acc)[4][2]) {
    const int tid = threadIdx.x, wid = rfl(tid >> 6), lane = tid & 63, wr = wid >> 2, wc = wid & 3, fr = lane & 15, fq = lane >> 4;
    const int c8 = (fq >> 1) | ((fq & 1) << 1);
#pragma unroll
    for (int m = 0; m < 4; ++m) {
        const f32x4 v0 = acc[m][0], v1 = acc[m][1];
        const auto r0 = __builtin_amdgcn_permlane16_swap(cvtpk(v0[0], v0[1]), cvtpk(v1[0], v1[1]), false, false);
        const auto r1 = __builtin_amdgcn_permlane16_swap(cvtpk(v0[2], v0[3]), cvtpk(v1[2], v1[3]), false, false);
        *(u32x4*)(outp + (size_t)(wr * 64 + m * 16 + fr) * 4096 + (size_t)(wc * 64 + c8 * 16)) = (u32x4){r0[0], r1[0], r0[1], r1[1]};
    }
}

template <int ROLE>
DI void oproj(ldsp lds, const char* ctx, const char* woB, const char* oa, const char* woA, char* part, unsigned* flag, char* outp) {
    const int tid = threadIdx.x, wid = rfl(tid >> 6), lane = tid & 63, wr = wid >> 2, wc = wid & 3, fr = lane & 15, fq = lane >> 4;
    constexpr int N1 = ROLE ? 64 - SPLIT0 : SPLIT0, NS = ROLE ? N1 + 16 : N1;
    unsigned voffA[2], voffW[2];
#pragma unroll
    for (int i = 0; i < 2; ++i) {
        voffA[i] = (unsigned)(tid * 16 + i * 8192);
        const int kg = (tid >> 7) + 4 * i, ng = (tid >> 5) & 3, r = (tid >> 2) & 7, c = (tid & 3) ^ (((tid >> 7) & 1) << 1);
        voffW[i] = (unsigned)(kg * 8 + r) * 2048u + (unsigned)(ng * 64 + c * 16);
    }
    const int aoff0 = (wr * 64 + fr) * 128 + ((fq ^ (fr & 7)) << 4), aoff1 = aoff0 ^ 64;
    const int boff0 = 16384 + (fq * 4 + wc) * 512 + ((lane >> 2) & 3) * 64 + (((lane & 3) * 8) ^ ((fq & 1) << 5)), boff1 = boff0 ^ 32;
    const __amdgpu_buffer_rsrc_t rs = __builtin_amdgcn_make_buffer_rsrc(part, 0, 65536, 0x00020000);
#define MPIECE(t, i) do { const int _t = (t) < NS ? (t) : NS - 1; const unsigned _b = ldsaddr_of(lds) + ((t) & 3) * 32768 + wid * 1024; \
        if (!ROLE || _t < N1) { \
            if ((i) < 2) glds16(ctx + (size_t)_t * 16384, voffA[(i) & 1], _b + ((i) & 1) * 8192); \
            else glds16(woB + (size_t)_t * 131072, voffW[(i) & 1], _b + 16384 + ((i) & 1) * 8192); \
        } else { \
            if ((i) < 2) glds16(oa + (size_t)(_t - N1) * 16384, voffA[(i) & 1], _b + ((i) & 1) * 8192); \
            else glds16(woA + (size_t)(_t - N1) * 131072, voffW[(i) & 1], _b + 16384 + ((i) & 1) * 8192); \
        } } while (0)
#define MFENCE do { asm volatile("" ::: "memory"); __builtin_amdgcn_sched_barrier(0); } while (0)
#define MRDA(AF, t, m) do { ldsp _b = lds + ((t) & 3) * 32768; AF[m][0] = ldsr(_b + aoff0 + (m) * 2048); AF[m][1] = ldsr(_b + aoff1 + (m) * 2048); } while (0)
#define MRDB(BF, t, k) do { ldsp _b = lds + ((t) & 3) * 32768; BF[0][k] = ldstr2(_b + boff0 + (k) * 8192); BF[1][k] = ldstr2(_b + boff1 + (k) * 8192); } while (0)
#define MM4(AF, BF, k, m0) do { acc[m0][0] = MFMA16(BF[0][k], AF[m0][k], acc[m0][0]); acc[m0][1] = MFMA16(BF[1][k], AF[m0][k], acc[m0][1]); \
        acc[(m0) + 1][0] = MFMA16(BF[0][k], AF[(m0) + 1][k], acc[(m0) + 1][0]); acc[(m0) + 1][1] = MFMA16(BF[1][k], AF[(m0) + 1][k], acc[(m0) + 1][1]); } while (0)
#define MSTEPX(AX, BX, AY, BY, t, VM, LD) do { \
        asm volatile("s_waitcnt vmcnt(" #VM ") lgkmcnt(0)\n\ts_barrier" ::: "memory"); MFENCE; \
        MRDB(BY, (t) + 1, 0); MRDA(AY, (t) + 1, 0); MM4(AX, BX, 0, 0); if (LD) MPIECE((t) + 4, 0); MFENCE; \
        MRDA(AY, (t) + 1, 1); MRDA(AY, (t) + 1, 2); MM4(AX, BX, 0, 2); if (LD) MPIECE((t) + 4, 1); MFENCE; \
        MRDB(BY, (t) + 1, 1); MRDA(AY, (t) + 1, 3); MM4(AX, BX, 1, 0); if (LD) MPIECE((t) + 4, 2); MFENCE; \
        MM4(AX, BX, 1, 2); if (LD) MPIECE((t) + 4, 3); MFENCE; \
    } while (0)
#define MSTEP(AX, BX, AY, BY, t) MSTEPX(AX, BX, AY, BY, t, 8, true)
    f32x4 acc[4][2];
#pragma unroll
    for (int m = 0; m < 4; ++m)
#pragma unroll
        for (int n = 0; n < 2; ++n) acc[m][n] = (f32x4){0.f, 0.f, 0.f, 0.f};
    bf16x8 A0[4][2], B0[2][2], A1[4][2], B1[2][2];
#pragma unroll
    for (int i = 0; i < 4; ++i) MPIECE(0, i);
#pragma unroll
    for (int i = 0; i < 4; ++i) MPIECE(1, i);
#pragma unroll
    for (int i = 0; i < 4; ++i) MPIECE(2, i);
#pragma unroll
    for (int i = 0; i < 4; ++i) MPIECE(3, i);
    WAIT_V(12); BAR;
    MRDB(B0, 0, 0); MRDB(B0, 0, 1); MRDA(A0, 0, 0); MRDA(A0, 0, 1); MRDA(A0, 0, 2); MRDA(A0, 0, 3);
    if constexpr (ROLE == 1) {
        for (int t = 0; t < N1; t += 2) { MSTEP(A0, B0, A1, B1, t); MSTEP(A1, B1, A0, B0, t + 1); }
        f32x4 hold[4][2];
#pragma unroll
        for (int m = 0; m < 4; ++m)
#pragma unroll
            for (int n = 0; n < 2; ++n) { hold[m][n] = acc[m][n]; acc[m][n] = (f32x4){0.f, 0.f, 0.f, 0.f}; }
#define PUB(m) do { __builtin_amdgcn_raw_buffer_store_b128(__builtin_bit_cast(u32x4, hold[m][0]), rs, ((m) * 2) * 8192 + tid * 16, 0, 16); \
                    __builtin_amdgcn_raw_buffer_store_b128(__builtin_bit_cast(u32x4, hold[m][1]), rs, ((m) * 2 + 1) * 8192 + tid * 16, 0, 16); MFENCE; } while (0)
        PUB(0); MSTEPX(A0, B0, A1, B1, N1, 10, true);     PUB(1); MSTEPX(A1, B1, A0, B0, N1 + 1, 12, true);
        PUB(2); MSTEPX(A0, B0, A1, B1, N1 + 2, 14, true); PUB(3); MSTEPX(A1, B1, A0, B0, N1 + 3, 14, true);
#undef PUB
        MSTEPX(A0, B0, A1, B1, N1 + 4, 12, true); MSTEPX(A1, B1, A0, B0, N1 + 5, 10, true);
        MSTEP(A0, B0, A1, B1, N1 + 6);
        if (tid == 0) __hip_atomic_store(flag, 1u, __ATOMIC_RELAXED, __HIP_MEMORY_SCOPE_AGENT);
        MFENCE;
        MSTEP(A1, B1, A0, B0, N1 + 7);
        for (int t = N1 + 8; t < NS - 4; t += 2) { MSTEP(A0, B0, A1, B1, t); MSTEP(A1, B1, A0, B0, t + 1); }
        MSTEPX(A0, B0, A1, B1, NS - 4, 8, false); MSTEPX(A1, B1, A0, B0, NS - 3, 4, false);
        MSTEPX(A0, B0, A1, B1, NS - 2, 0, false); MSTEPX(A1, B1, A0, B0, NS - 1, 0, false);
        store_out(outp, acc);
    } else {
        for (int t = 0; t < NS - 6; t += 2) { MSTEP(A0, B0, A1, B1, t); MSTEP(A1, B1, A0, B0, t + 1); }
        MSTEP(A0, B0, A1, B1, NS - 6);
        if (wid == 0) { while (rfl((int)__hip_atomic_load(flag, __ATOMIC_RELAXED, __HIP_MEMORY_SCOPE_AGENT)) != 1) __builtin_amdgcn_s_sleep(2); }
        MFENCE;
        MSTEP(A1, B1, A0, B0, NS - 5);
        u32x4 pp[4][2];
#pragma unroll
        for (int m = 0; m < 4; ++m)
#pragma unroll
            for (int n = 0; n < 2; ++n) pp[m][n] = __builtin_amdgcn_raw_buffer_load_b128(rs, (m * 2 + n) * 8192 + tid * 16, 0, 16);
        MFENCE;
        MSTEPX(A0, B0, A1, B1, NS - 4, 16, false); MSTEPX(A1, B1, A0, B0, NS - 3, 12, false);
        MSTEPX(A0, B0, A1, B1, NS - 2, 8, false);  MSTEPX(A1, B1, A0, B0, NS - 1, 8, false);
#pragma unroll
        for (int m = 0; m < 4; ++m)
#pragma unroll
            for (int n = 0; n < 2; ++n) acc[m][n] += __builtin_bit_cast(f32x4, pp[m][n]);
        store_out(outp, acc);
    }
    WAIT_V(0);
#undef MPIECE
#undef MFENCE
#undef MRDA
#undef MRDB
#undef MM4
#undef MSTEP
#undef MSTEPX
}

__global__ __launch_bounds__(512, 2) void k_oproj(const char* __restrict__ woA, const char* __restrict__ woB, char* __restrict__ ws, char* __restrict__ out) {
    extern __shared__ __attribute__((aligned(16))) unsigned char smem[];
    ldsp lds = (ldsp)smem;
    const int b = blockIdx.x, xcd = b & 7, idx = b >> 3, role = xcd >> 2;
    const int rt = 4 * (xcd & 3) + (idx & 3), ct = idx >> 2, tile = rt * 8 + ct;
    const char* ctx = ws + WS_CTX + (size_t)rt * 128 * 8192;
    unsigned* flag = (unsigned*)(ws + WS_FLAG + tile * 128);
    char* part = ws + WS_PART + (size_t)tile * 65536;
    STAMP(ws, 2, 0);
    if (role == 1) oproj<1>(lds, ctx + SPLIT0 * 16384, woB + (size_t)SPLIT0 * 131072 + ct * 256, ws + WS_OA + (size_t)rt * 128 * 2048, woA + ct * 256, part, flag, out + (size_t)rt * 128 * 4096 + ct * 256);
    else oproj<0>(lds, ctx, woB + ct * 256, nullptr, nullptr, part, flag, out + (size_t)rt * 128 * 4096 + 2048 + ct * 256);
    STAMP(ws, 2, 1 + role);
}

extern "C" void kernel_launch(void* const* d_in, const int* in_sizes, int n_in, void* d_out, int out_size, void* d_ws, size_t ws_size, hipStream_t stream) {
    (void)in_sizes; (void)n_in; (void)out_size; (void)ws_size;
    constexpr int LDS1 = 163840, LDS2 = 73728 + 4096, LDS3 = 131072;
    (void)hipFuncSetAttribute((const void*)k_proj, hipFuncAttributeMaxDynamicSharedMemorySize, LDS1);
    (void)hipFuncSetAttribute((const void*)k_attn, hipFuncAttributeMaxDynamicSharedMemorySize, LDS2);
    (void)hipFuncSetAttribute((const void*)k_oproj, hipFuncAttributeMaxDynamicSharedMemorySize, LDS3);
    char* ws = (char*)d_ws;
    k_proj<<<256, 512, LDS1, stream>>>((const char*)d_in[0], (const char*)d_in[1], (const char*)d_in[2], (const char*)d_in[3], (const char*)d_in[5],
                                       (const char*)d_in[6], (const char*)d_in[7], (const char*)d_in[8], (const char*)d_in[9], ws);
    k_attn<<<512, 256, LDS2, stream>>>(ws, (const char*)d_in[4], (const char*)d_in[10], (const char*)d_in[0], (const char*)d_in[1]);
    k_oproj<<<256, 512, LDS3, stream>>>((const char*)d_in[4], (const char*)d_in[10], ws, (char*)d_out);
}
```

```cpp
#include <hip/hip_runtime.h>
#include <stdint.h>

#define LAS __attribute__((address_space(3)))
#define DI __device__ __forceinline__
typedef unsigned short u16;
typedef short bf16x8 __attribute__((ext_vector_type(8)));
typedef short s16x4 __attribute__((ext_vector_type(4)));
typedef float f32x4 __attribute__((ext_vector_type(4)));
typedef float f32x16 __attribute__((ext_vector_type(16)));
typedef unsigned u32x4 __attribute__((ext_vector_type(4)));
typedef unsigned u32x2 __attribute__((ext_vector_type(2)));
typedef LAS unsigned char* ldsp;

constexpr size_t WS_QB = 0;
constexpr size_t WS_KB = WS_QB + (16u << 20);
constexpr size_t WS_VB = WS_KB + (16u << 20);
constexpr size_t WS_KA = WS_VB + (16u << 20);
constexpr size_t WS_VA = WS_KA + (128u << 10);
constexpr size_t WS_CTX = WS_VA + (128u << 10);
constexpr size_t WS_OA = WS_CTX + (16u << 20);
constexpr size_t WS_PART = WS_KB;
constexpr size_t WS_FLAG = WS_OA + (4u << 20);

constexpr float LOG2E = 1.4426950408889634f;
constexpr float SCALE_A = 0.17677669529663687f * LOG2E;
constexpr float SCALE_B = 0.08838834764831845f * LOG2E;
constexpr float THR = 40.f;

#define WAIT_V(n) asm volatile("s_waitcnt vmcnt(" #n ")" ::: "memory")
#define WAIT_L(n) asm volatile("s_waitcnt lgkmcnt(" #n ")" ::: "memory")
#define BAR __builtin_amdgcn_s_barrier()
#define BARF do { asm volatile("" ::: "memory"); __builtin_amdgcn_s_barrier(); asm volatile("" ::: "memory"); } while (0)
#define SCHED __builtin_amdgcn_sched_barrier(0)
#define MFMA16(a, b, c) __builtin_amdgcn_mfma_f32_16x16x32_bf16(a, b, c, 0, 0, 0)
#define MFMA32(a, b, c) __builtin_amdgcn_mfma_f32_32x32x16_bf16(a, b, c, 0, 0, 0)

typedef float f32x2 __attribute__((ext_vector_type(2)));
typedef __bf16 bf16x2_t __attribute__((ext_vector_type(2)));
DI unsigned cvtpk(float lo, float hi) { const f32x2 v = {lo, hi}; return __builtin_bit_cast(unsigned, __builtin_convertvector(v, bf16x2_t)); }
DI void glds16(const char* sbase, unsigned voff, unsigned ldsaddr) {
    asm volatile("s_mov_b32 m0, %2\n\ts_nop 0\n\tglobal_load_lds_dwordx4 %0, %1" :: "v"(voff), "s"(sbase), "s"(ldsaddr) : "memory");
}
template <int OFF> DI void glds16o(const char* sbase, unsigned voff, unsigned ldsaddr) {
    asm volatile("s_mov_b32 m0, %2\n\ts_nop 0\n\tglobal_load_lds_dwordx4 %0, %1 offset:%3" :: "v"(voff), "s"(sbase), "s"(ldsaddr), "i"(OFF) : "memory");
}
DI unsigned ldsaddr_of(ldsp p) { return (unsigned)(size_t)p; }
DI bf16x8 ldsr(ldsp p) { return *(const LAS bf16x8*)p; }
DI s16x4 ldstr(ldsp p) { return __builtin_amdgcn_ds_read_tr16_b64_v4i16((LAS s16x4*)p); }
DI bf16x8 ldstr2(ldsp p) { const s16x4 a = ldstr(p), b = ldstr(p + 256); return (bf16x8){a[0], a[1], a[2], a[3], b[0], b[1], b[2], b[3]}; }
DI int rfl(int x) { return __builtin_amdgcn_readfirstlane(x); }

DI void warm(const char* base, int npieces, unsigned dump) {
    const int wid = rfl(threadIdx.x >> 6); const unsigned l16 = (threadIdx.x & 63) * 16;
    for (int i = 0; i < npieces; ++i) glds16(base + (size_t)i * 8192 + wid * 1024, l16, dump + wid * 1024);
}

constexpr int HTB = 16384;
#define SA(b, h) (((b) * 2 + (h)) * HTB)
#define SB(b, h) ((4 + (b) * 2 + (h)) * HTB)

template <bool SWAP, bool KVT>
DI void gemm_big(ldsp lds, const char* gA, unsigned lda2, const char* gW, unsigned ldw2, int nt, int nunits,
                 char* out, int row0, int head0, float scale, const char* wk, const char* wv, const char* xkv) {
    const int tid = threadIdx.x, wid = rfl(tid >> 6), lane = tid & 63, wr = wid >> 2, wc = wid & 3, fr = lane & 15, fq = lane >> 4;
    unsigned voffA[2], voffW[2];
#pragma unroll
    for (int i = 0; i < 2; ++i) {
        voffA[i] = (unsigned)((tid >> 3) + 64 * i) * lda2 + (unsigned)(((tid & 7) ^ ((tid >> 3) & 7)) << 4);
        const int kg = (tid >> 7) + 4 * i, ng = (tid >> 5) & 3, r = (tid >> 2) & 7, c = (tid & 3) ^ (((tid >> 7) & 1) << 1);
        voffW[i] = (unsigned)(kg * 8 + r) * ldw2 + (unsigned)(ng * 64 + c * 16);
    }
    const size_t kstepA = 128, kstepW = (size_t)64 * ldw2, hstepA = (size_t)128 * lda2, hstepW = 256;
    const unsigned ldsw = ldsaddr_of(lds) + (unsigned)wid * 1024u;
    const int rowb = wr * 64 + (SWAP ? 16 * (fr >> 3) + (fr & 7) : fr);
    const int aoff0 = rowb * 128 + ((fq ^ (fr & 7)) << 4), aoff1 = aoff0 ^ 64;
    const int boff0 = (fq * 4 + wc) * 512 + ((lane >> 2) & 3) * 64 + (((lane & 3) * 8) ^ ((fq & 1) << 5)), boff1 = boff0 ^ 32;
#define MOFF(m) (SWAP ? ((m) >> 1) * 4096 + ((m) & 1) * 1024 : (m) * 2048)
#define STAGE(bufoff, gbase, voff) do { _Pragma("unroll") for (int _i = 0; _i < 2; ++_i) glds16((gbase), (voff)[_i], (bufoff) + ldsw + _i * 8192); } while (0)
    const unsigned voffkv = (unsigned)((lane >> 2) * 64 + (((lane & 3) ^ (((lane >> 5) & 1) << 1)) << 4));
#define KVSTAGE(bufoff) do { const char* _s = (((bufoff) >> 16) ? wv : wk) + (size_t)wid * 8192 + (((bufoff) >> 14) & 3) * 2048; \
        glds16(_s, voffkv, (bufoff) + ldsw); glds16(_s + 1024, voffkv, (bufoff) + ldsw + 8192); } while (0)
#define STAGE2(KVL, bufoff, gbase, voff) do { if (KVL) KVSTAGE(bufoff); else STAGE(bufoff, gbase, voff); } while (0)
#define LDA(dst, b, h) do { _Pragma("unroll") for (int m = 0; m < 4; ++m) { dst[m][0] = ldsr(lds + SA(b, h) + aoff0 + MOFF(m)); dst[m][1] = ldsr(lds + SA(b, h) + aoff1 + MOFF(m)); } } while (0)
#define LDB(dst, b, h) do { _Pragma("unroll") for (int k = 0; k < 2; ++k) { dst[0][k] = ldstr2(lds + SB(b, h) + boff0 + k * 8192); dst[1][k] = ldstr2(lds + SB(b, h) + boff1 + k * 8192); } } while (0)
#define MMA(ai, bj, At, Bt) do { __builtin_amdgcn_s_setprio(1); _Pragma("unroll") for (int m = 0; m < 4; ++m) _Pragma("unroll") for (int n = 0; n < 2; ++n) _Pragma("unroll") for (int k = 0; k < 2; ++k) \
        acc[ai][bj][m][n] = SWAP ? MFMA16(At[m][k], Bt[n][k], acc[ai][bj][m][n]) : MFMA16(Bt[n][k], At[m][k], acc[ai][bj][m][n]); __builtin_amdgcn_s_setprio(0); } while (0)
    f32x4 acc[2][2][4][2];
#pragma unroll
    for (int a = 0; a < 2; ++a)
#pragma unroll
        for (int b = 0; b < 2; ++b)
#pragma unroll
            for (int m = 0; m < 4; ++m)
#pragma unroll
                for (int n = 0; n < 2; ++n) acc[a][b][m][n] = (f32x4){0.f, 0.f, 0.f, 0.f};
    bf16x8 At[4][2], B0[2][2], B1[2][2];
    const char* cA = gA; const char* cW = gW;
    STAGE(SB(0, 0), cW, voffW); STAGE(SA(0, 0), cA, voffA); STAGE(SB(0, 1), cW + hstepW, voffW); STAGE(SA(0, 1), cA + hstepA, voffA);
    if (wr == 1) BAR;
    WAIT_V(4); BAR;
    STAGE(SB(1, 0), cW + kstepW, voffW); STAGE(SA(1, 0), cA + kstepA, voffA); STAGE(SB(1, 1), cW + hstepW + kstepW, voffW);
    WAIT_V(6); BAR;
    for (int ui = 0;; ++ui) {
        const bool has_next = ui + 1 < nunits;
        const char* nW = has_next ? cW + 512 : cW;
#define KBODY(t, KVL) do { \
            const bool last = (t == nt - 2); \
            const char* a1 = cA + (size_t)(t + 1) * kstepA; \
            const char* a2 = last ? cA : cA + (size_t)(t + 2) * kstepA; const char* b2 = last ? nW : cW + (size_t)(t + 2) * kstepW; \
            const char* a3 = a2 + kstepA; const char* b3 = b2 + kstepW; \
            const bool fresh = (ui > 0 && t == 0); \
            LDB(B0, 0, 0); SCHED; LDA(At, 0, 0); if (!fresh) STAGE(SA(1, 1), a1 + hstepA, voffA); \
            WAIT_L(8); BAR; WAIT_L(0); MMA(0, 0, At, B0); BAR; SCHED; \
            LDB(B1, 0, 1); STAGE2(KVL, SB(0, 0), b2, voffW); \
            BAR; WAIT_L(0); MMA(0, 1, At, B1); BAR; \
            LDA(At, 0, 1); STAGE2(KVL, SA(0, 0), a2, voffA); \
            BAR; WAIT_L(0); MMA(1, 0, At, B0); BAR; SCHED; \
            STAGE2(KVL, SB(0, 1), b2 + hstepW, voffW); \
            if (fresh) WAIT_V(22); else WAIT_V(6); \
            BAR; MMA(1, 1, At, B1); BAR; \
            LDB(B0, 1, 0); SCHED; LDA(At, 1, 0); STAGE2(KVL, SA(0, 1), a2 + hstepA, voffA); \
            WAIT_L(8); BAR; WAIT_L(0); MMA(0, 0, At, B0); BAR; SCHED; \
            LDB(B1, 1, 1); STAGE2(KVL, SB(1, 0), b3, voffW); \
            BAR; WAIT_L(0); MMA(0, 1, At, B1); BAR; \
            LDA(At, 1, 1); STAGE2(KVL, SA(1, 0), a3, voffA); \
            BAR; WAIT_L(0); MMA(1, 0, At, B0); BAR; SCHED; \
            STAGE2(KVL, SB(1, 1), b3 + hstepW, voffW); \
            WAIT_V(6); BAR; MMA(1, 1, At, B1); BAR; \
        } while (0)
        for (int t = 0; t < (KVT ? nt - 2 : nt); t += 2) KBODY(t, false);
        if constexpr (KVT) KBODY(nt - 2, true);
        if (has_next) STAGE(SA(1, 1), cA + kstepA + hstepA, voffA);
        if (KVT) { KVSTAGE(SA(1, 1));
            const unsigned vx = (unsigned)(fr * 2048 + fq * 16);
#pragma unroll
            for (int k = 0; k < 4; ++k) glds16(xkv + wid * 256 + k * 64, vx, ldsaddr_of(lds) + 131072 + wid * 4096 + k * 1024); }
        const int h0 = head0 + 2 * ui;
        if constexpr (!SWAP) {
#pragma unroll
            for (int ai = 0; ai < 2; ++ai)
#pragma unroll
                for (int m = 0; m < 4; ++m) {
                    const int t = row0 + ai * 128 + wr * 64 + m * 16 + fr;
#pragma unroll
                    for (int bj = 0; bj < 2; ++bj) {
                        const f32x4 v0 = acc[ai][bj][m][0] * scale, v1 = acc[ai][bj][m][1] * scale;
                        const auto r0 = __builtin_amdgcn_permlane16_swap(cvtpk(v0[0], v0[1]), cvtpk(v1[0], v1[1]), false, false);
                        const auto r1 = __builtin_amdgcn_permlane16_swap(cvtpk(v0[2], v0[3]), cvtpk(v1[2], v1[3]), false, false);
                        const u32x4 w = {r0[0], r1[0], r0[1], r1[1]};
                        const size_t off = ((size_t)(((h0 + bj) * 64 + (t >> 5)) * 8 + 2 * wc + (fq & 1))) * 1024 + (size_t)(((t & 31) + 32 * (fq >> 1)) * 16);
                        *(u32x4*)(out + off) = w;
                    }
                }
        } else {
#pragma unroll
            for (int ai = 0; ai < 2; ++ai)
#pragma unroll
                for (int mp = 0; mp < 2; ++mp) {
                    const int pos = row0 + ai * 128 + wr * 64 + 32 * mp + 8 * fq;
#pragma unroll
                    for (int bj = 0; bj < 2; ++bj)
#pragma unroll
                        for (int n = 0; n < 2; ++n) {
                            const f32x4 a = acc[ai][bj][2 * mp][n], b = acc[ai][bj][2 * mp + 1][n];
                            const u32x4 w = {cvtpk(a[0], a[1]), cvtpk(a[2], a[3]), cvtpk(b[0], b[1]), cvtpk(b[2], b[3])};
                            const size_t off = ((size_t)(((h0 + bj) * 128 + (pos >> 4)) * 4 + wc)) * 1024 + (size_t)((n * 16 + fr + 32 * (fq & 1)) * 16);
                            *(u32x4*)(out + off) = w;
                        }
                }
        }
        if (!has_next) break;
#pragma unroll
        for (int a = 0; a < 2; ++a)
#pragma unroll
            for (int b = 0; b < 2; ++b)
#pragma unroll
                for (int m = 0; m < 4; ++m)
#pragma unroll
                    for (int n = 0; n < 2; ++n) acc[a][b][m][n] = (f32x4){0.f, 0.f, 0.f, 0.f};
        cW = nW;
    }
    WAIT_V(16);
    if (wr == 0) BAR;
    BARF;
#undef MOFF
#undef KVSTAGE
#undef KBODY
#undef STAGE2
#undef STAGE
#undef LDA
#undef LDB
#undef MMA
}

DI void gemm_kv(ldsp lds, const char* xA, const char* wk, const char* wv, int r16, char* ws) {
    const int tid = threadIdx.x, wid = rfl(tid >> 6), lane = tid & 63, fr = lane & 15, fq = lane >> 4;
    bf16x8 xf[4];
#pragma unroll
    for (int k = 0; k < 4; ++k) xf[k] = ldsr(lds + 131072 + wid * 4096 + k * 1024 + lane * 16);
    ldsp wb = lds + wid * 1024 + (fq >> 1) * 8192 + (fq & 1) * 512 + ((lane >> 2) & 3) * 64;
    const int c0 = ((lane & 3) * 8) ^ ((fq & 1) << 5);
    f32x4 acc[4];
#pragma unroll
    for (int n = 0; n < 4; ++n) acc[n] = (f32x4){0.f, 0.f, 0.f, 0.f};
#pragma unroll
    for (int k = 0; k < 4; ++k)
#pragma unroll
        for (int n = 0; n < 4; ++n) acc[n] = MFMA16(ldstr2(wb + ((n >> 1) * 4 + k) * 16384 + (c0 ^ ((n & 1) << 5))), xf[k], acc[n]);
    WAIT_L(0); BARF;
#pragma unroll
    for (int n = 0; n < 4; ++n) *(LAS f32x4*)(lds + (n * 8 + wid) * 1024 + lane * 16) = acc[n];
    WAIT_L(0); BARF;
    if (wid < 4) {
        f32x4 v = *(const LAS f32x4*)(lds + (wid * 8) * 1024 + lane * 16);
#pragma unroll
        for (int w = 1; w < 8; ++w) v += *(const LAS f32x4*)(lds + (wid * 8 + w) * 1024 + lane * 16);
        const int t = r16 * 16 + fr;
        const unsigned w0 = cvtpk(v[0], v[1]), w1 = cvtpk(v[2], v[3]);
        if (wid < 2) {
            const size_t off = ((size_t)((t >> 5) * 2 + wid)) * 1024 + (size_t)(((t & 31) + 32 * (fq & 1)) * 16 + (fq >> 1) * 8);
            *(u32x2*)(ws + WS_KA + off) = (u32x2){w0, w1};
        } else {
            const int pos = (t & ~12) | ((t & 4) << 1) | ((t & 8) >> 1);
            const int d = (wid - 2) * 16 + 4 * fq;
            char* p = ws + WS_VA + (size_t)(pos >> 4) * 1024 + (size_t)((d + 32 * ((pos >> 3) & 1)) * 16 + (pos & 7) * 2);
            *(u16*)(p) = (u16)w0; *(u16*)(p + 16) = (u16)(w0 >> 16); *(u16*)(p + 32) = (u16)w1; *(u16*)(p + 48) = (u16)(w1 >> 16);
        }
    }
}

__global__ __launch_bounds__(512, 2) void k_proj(const char* __restrict__ xA, const char* __restrict__ wqA, const char* __restrict__ wkA, const char* __restrict__ wvA,
                                                 const char* __restrict__ xB, const char* __restrict__ ckv, const char* __restrict__ wqB,
                                                 const char* __restrict__ wuk, const char* __restrict__ wuv, char* __restrict__ ws) {
    extern __shared__ __attribute__((aligned(16))) unsigned char smem[];
    ldsp lds = (ldsp)smem;
    const int b = blockIdx.x, xcd = b & 7, idx = b >> 3;
    if (b == 0 && threadIdx.x < 128) *(unsigned*)(ws + WS_FLAG + threadIdx.x * 128) = 0u;
    warm(xA + (size_t)b * 16384, 2, ldsaddr_of(lds) + 131072);
    warm(wqA + (size_t)b * 8192, 1, ldsaddr_of(lds) + 131072);
    if (idx < 16) warm((idx < 8 ? wkA : wvA) + (size_t)(idx & 7) * 8192, 1, ldsaddr_of(lds) + 131072);
    if (idx < 16) {
        const int rt = 4 * (xcd & 1) + (idx & 3), ct = 4 * (xcd >> 1) + (idx >> 2);
        gemm_big<false, true>(lds, xB + (size_t)rt * 256 * 2048, 2048u, wqB + (size_t)ct * 512, 8192u, 16, 1, ws + WS_QB, rt * 256, ct * 2, SCALE_B, wkA, wvA, xA + (size_t)(xcd * 16 + idx) * 16 * 2048);
        gemm_kv(lds, xA, wkA, wvA, xcd * 16 + idx, ws);
    } else if (idx < 24) {
        const int i = idx - 16, rt = 4 * (xcd & 1) + (i & 3), cp = 2 * (xcd >> 1) + (i >> 2);
        gemm_big<false, false>(lds, ckv + (size_t)rt * 256 * 1024, 1024u, wuk + (size_t)cp * 1024, 8192u, 8, 2, ws + WS_KB, rt * 256, cp * 4, 1.f, nullptr, nullptr, nullptr);
    } else {
        const int i = idx - 24, rt = 4 * (xcd & 1) + (i & 3), cp = 2 * (xcd >> 1) + (i >> 2);
        gemm_big<true, false>(lds, ckv + (size_t)rt * 256 * 1024, 1024u, wuv + (size_t)cp * 1024, 8192u, 8, 2, ws + WS_VB, rt * 256, cp * 4, 1.f, nullptr, nullptr, nullptr);
    }
}

DI float rowmax16(const f32x16& p) { float a = fmaxf(p[0], p[1]); _Pragma("unroll") for (int r = 2; r < 16; r += 2) a = fmaxf(fmaxf(a, p[r]), p[r + 1]); return a; }
constexpr int APRIO_OFF = 4;
DI float swap32max(float m) { const auto rr = __builtin_amdgcn_permlane32_swap(__float_as_uint(m), __float_as_uint(m), false, false); return fmaxf(__uint_as_float(rr[0]), __uint_as_float(rr[1])); }
DI float swap32sum(float m) { const auto rr = __builtin_amdgcn_permlane32_swap(__float_as_uint(m), __float_as_uint(m), false, false); return __uint_as_float(rr[0]) + __uint_as_float(rr[1]); }
DI bf16x8 packp(const f32x16& p, int b) { const u32x4 w = {cvtpk(p[b], p[b + 1]), cvtpk(p[b + 2], p[b + 3]), cvtpk(p[b + 4], p[b + 5]), cvtpk(p[b + 6], p[b + 7])}; return __builtin_bit_cast(bf16x8, w); }
DI f32x16 splat16(float v) { f32x16 r; _Pragma("unroll") for (int i = 0; i < 16; ++i) r[i] = v; return r; }

DI void attn_b_store(char* ws, const f32x16 (&o)[4], float l, int row, int head, int hh) {
    const float inv = 1.f / swap32sum(l);
    char* orow = ws + WS_CTX + (size_t)((row >> 7) * 64 + 2 * head) * 16384 + (size_t)(row & 127) * 128;
#pragma unroll
    for (int dd = 0; dd < 4; ++dd) {
        unsigned w[8];
#pragma unroll
        for (int i = 0; i < 4; ++i) { w[2 * i] = cvtpk(o[dd][4 * i] * inv, o[dd][4 * i + 1] * inv); w[2 * i + 1] = cvtpk(o[dd][4 * i + 2] * inv, o[dd][4 * i + 3] * inv); }
#pragma unroll
        for (int k = 0; k < 4; k += 2) {
            const auto x0 = __builtin_amdgcn_permlane32_swap(w[2 * k], w[2 * k + 2], false, false);
            const auto x1 = __builtin_amdgcn_permlane32_swap(w[2 * k + 1], w[2 * k + 3], false, false);
            *(u32x4*)(orow + (dd >> 1) * 16384 + ((((dd & 1) * 4 + k + hh) ^ (row & 7)) << 4)) = (u32x4){x0[0], x1[0], x0[1], x1[1]};
        }
    }
}

DI void attn_b(ldsp lds, char* ws, int head, int j) {
    const int tid = threadIdx.x, wq = rfl(tid >> 6), lane = tid & 63, r32 = lane & 31, hh = lane >> 5;
    const char* Kg = ws + WS_KB + (size_t)head * (512u << 10) + (size_t)wq * 4096;
    const char* Vg = ws + WS_VB + (size_t)head * (512u << 10) + (size_t)wq * 4096;
    const char* Qg = ws + WS_QB + (size_t)head * (512u << 10) + lane * 16;
    const unsigned l16 = lane * 16, l0 = ldsaddr_of(lds) + wq * 4096;
    const int NA = 32 - 2 * j;
    ldsp kbase = lds + lane * 16, vbase = lds + 32768 + lane * 16;
#define KT(x) ((x) < NA ? (x) : ((x) > 33 ? 0 : (x) - NA))
#define KPIECE(x, i) glds16o<(i) * 1024>(Kg + (size_t)KT(x) * 16384, l16, l0 + ((x) & 1) * 16384)
#define VPIECE(x, i) glds16o<(i) * 1024>(Vg + (size_t)KT(x) * 16384, l16, l0 + 32768 + ((x) & 1) * 16384)
#define KFR(x, h, i) ldsr(kbase + ((x) & 1) * 16384 + (h) * 8192 + (i) * 1024)
#define VFR(x, h, i) ldsr(vbase + ((x) & 1) * 16384 + (h) * 8192 + (i) * 1024)
    bf16x8 qf[8], kf[8], vf[8];
    KPIECE(0, 0); KPIECE(0, 1); KPIECE(0, 2); KPIECE(0, 3);
    VPIECE(0, 0); VPIECE(0, 1); VPIECE(0, 2); VPIECE(0, 3);
    KPIECE(1, 0); KPIECE(1, 1); KPIECE(1, 2); KPIECE(1, 3);
#pragma unroll
    for (int s = 0; s < 8; ++s) qf[s] = *(const bf16x8*)(Qg + (size_t)(((15 - j) * 4 + wq) * 8 + s) * 1024);
    asm volatile("" :: "v"(qf[0]), "v"(qf[1]), "v"(qf[2]), "v"(qf[3]), "v"(qf[4]), "v"(qf[5]), "v"(qf[6]), "v"(qf[7]));
    WAIT_V(0); BARF;
    f32x16 o[4]; o[0] = splat16(0.f); o[1] = o[0]; o[2] = o[0]; o[3] = o[0];
    f32x16 sA, sB, negm;
    float mref, l = 0.f;
    {
#pragma unroll
        for (int i = 0; i < 8; ++i) kf[i] = KFR(0, 0, i);
        sA = splat16(0.f);
#pragma unroll
        for (int i = 0; i < 8; ++i) sA = MFMA32(kf[i], qf[i], sA);
#pragma unroll
        for (int i = 0; i < 8; ++i) kf[i] = KFR(0, 1, i);
        float m = fmaxf(sA[0], sA[1]);
#pragma unroll
        for (int r = 2; r < 16; r += 2) m = fmaxf(fmaxf(m, sA[r]), sA[r + 1]);
        mref = swap32max(m);
        sA -= mref; negm = splat16(-mref); asm volatile("" : "+v"(negm));
    }
    u32x4 pw0, pw1;
#define EXPL(A, e) do { A[e] = __builtin_amdgcn_exp2f(A[e]); l += A[e]; } while (0)
#define SM2(A, e, PW, w) do { EXPL(A, e); EXPL(A, (e) + 1); PW[w] = cvtpk(A[e], A[(e) + 1]); asm volatile("" : "+v"(PW), "+v"(l)); } while (0)
#define PK(A, e, PW, w) do { PW[w] = cvtpk(A[e], A[(e) + 1]); asm volatile("" : "+v"(PW), "+v"(l)); } while (0)
#define PINL asm volatile("" : "+v"(l))
#define FENCE do { asm volatile("" ::: "memory"); __builtin_amdgcn_sched_barrier(0); } while (0)
#define PF(k) __builtin_bit_cast(bf16x8, pw##k)
#define MX(a, b, c, d) do { ma = fmaxf(fmaxf(ma, a), b); mb = fmaxf(fmaxf(mb, c), d); asm volatile("" : "+v"(ma), "+v"(mb)); } while (0)
#define EX(A, e) do { A[e] = __builtin_amdgcn_exp2f(A[e]); asm volatile("" : "+v"(A[e])); } while (0)
#define AD(A, e) do { l += A[e]; } while (0)
#define HSTEP(A, B, u, H, QRL, DEC) do { \
        const int xn = (u) + (H), hn = (H) ^ 1;                \
        if (!(H)) { asm volatile("s_waitcnt vmcnt(0) lgkmcnt(0)\n\ts_barrier" ::: "memory"); FENCE; } \
        vf[0] = VFR(u, H, 0); B = MFMA32(kf[0], qf[0], negm); asm volatile("" : "+v"(B)); if (!(H)) VPIECE((u) + 1, 3); EX(A, 0); EX(A, 1); FENCE; \
        vf[1] = VFR(u, H, 1); B = MFMA32(kf[1], qf[1], B); asm volatile("" : "+v"(B));    AD(A, 0); AD(A, 1); PK(A, 0, pw0, 0); EX(A, 2); FENCE; \
        vf[2] = VFR(u, H, 2); B = MFMA32(kf[2], qf[2], B); asm volatile("" : "+v"(B));    if (!(H)) VPIECE((u) + 1, 0); AD(A, 2); EX(A, 3); PINL; FENCE; \
        vf[3] = VFR(u, H, 3); B = MFMA32(kf[3], qf[3], B); asm volatile("" : "+v"(B));    AD(A, 3); PK(A, 2, pw0, 1); EX(A, 4); FENCE; \
        vf[4] = VFR(u, H, 4); B = MFMA32(kf[4], qf[4], B); asm volatile("" : "+v"(B));    if (!(H)) VPIECE((u) + 1, 1); AD(A, 4); EX(A, 5); PINL; FENCE; \
        vf[5] = VFR(u, H, 5); B = MFMA32(kf[5], qf[5], B); asm volatile("" : "+v"(B));    AD(A, 5); PK(A, 4, pw0, 2); EX(A, 6); FENCE; \
        vf[6] = VFR(u, H, 6); B = MFMA32(kf[6], qf[6], B); asm volatile("" : "+v"(B));    if (!(H)) VPIECE((u) + 1, 2); AD(A, 6); EX(A, 7); PINL; FENCE; \
        vf[7] = VFR(u, H, 7); B = MFMA32(kf[7], qf[7], B); asm volatile("" : "+v"(B));    AD(A, 7); PK(A, 6, pw0, 3); EX(A, 8); EX(A, 9); FENCE; \
        if (QRL) {      \
            const char* qn = ws + WS_QB + (size_t)head * (512u << 10) + (size_t)(j * 4 + wq) * 8192; \
            asm volatile("global_load_dwordx4 %0, %8, %9\n\tglobal_load_dwordx4 %1, %8, %9 offset:1024\n\tglobal_load_dwordx4 %2, %8, %9 offset:2048\n\tglobal_load_dwordx4 %3, %8, %9 offset:3072\n\t" \
                         "global_load_dwordx4 %4, %8, %10\n\tglobal_load_dwordx4 %5, %8, %10 offset:1024\n\tglobal_load_dwordx4 %6, %8, %10 offset:2048\n\tglobal_load_dwordx4 %7, %8, %10 offset:3072" \
                         : "+v"(qf[0]), "+v"(qf[1]), "+v"(qf[2]), "+v"(qf[3]), "+v"(qf[4]), "+v"(qf[5]), "+v"(qf[6]), "+v"(qf[7]) : "v"(l16), "s"(qn), "s"(qn + 4096) : "memory"); \
        } \
        kf[0] = KFR((u) + 1, H, 0); kf[1] = KFR((u) + 1, H, 1); o[0] = MFMA32(vf[0], PF(0), o[0]); asm volatile("" : "+v"(o[0])); AD(A, 8); AD(A, 9); PK(A, 8, pw1, 0); EX(A, 10); EX(A, 11); FENCE; \
        kf[2] = KFR((u) + 1, H, 2); kf[3] = KFR((u) + 1, H, 3); o[1] = MFMA32(vf[1], PF(0), o[1]); asm volatile("" : "+v"(o[1])); AD(A, 10); AD(A, 11); PK(A, 10, pw1, 1); EX(A, 12); EX(A, 13); FENCE; \
        kf[4] = KFR((u) + 1, H, 4); kf[5] = KFR((u) + 1, H, 5); o[2] = MFMA32(vf[2], PF(0), o[2]); asm volatile("" : "+v"(o[2])); AD(A, 12); AD(A, 13); PK(A, 12, pw1, 2); EX(A, 14); EX(A, 15); FENCE; \
        kf[6] = KFR((u) + 1, H, 6); kf[7] = KFR((u) + 1, H, 7); o[3] = MFMA32(vf[3], PF(0), o[3]); asm volatile("" : "+v"(o[3])); AD(A, 14); AD(A, 15); PK(A, 14, pw1, 3); FENCE; \
        { const int dt = (xn < NA ? NA : 34) - xn;             \
          if (dt <= 2) { \
            const int dq = 32 * wq + r32 - 64 * (2 - dt) - 32 * hn - 4 * hh; \
            _Pragma("unroll") for (int r = 0; r < 16; ++r) { const int c = (r & 3) + 8 * (r >> 2); if (c > dq) B[r] = -INFINITY; } \
        } } \
        float ma = fmaxf(fmaxf(B[0], B[1]), B[2]), mb = fmaxf(fmaxf(B[3], B[4]), B[5]); \
        o[0] = MFMA32(vf[4], PF(1), o[0]); asm volatile("" : "+v"(o[0])); if (!(H)) KPIECE((u) + 2, 0); MX(B[6], B[7], B[8], B[9]); FENCE; \
        o[1] = MFMA32(vf[5], PF(1), o[1]); asm volatile("" : "+v"(o[1])); if (!(H)) KPIECE((u) + 2, 1); MX(B[10], B[11], B[12], B[13]); FENCE; \
        o[2] = MFMA32(vf[6], PF(1), o[2]); asm volatile("" : "+v"(o[2])); if (!(H)) KPIECE((u) + 2, 2); ma = fmaxf(fmaxf(ma, B[14]), B[15]); ma = fmaxf(ma, mb); asm volatile("" : "+v"(ma)); FENCE; \
        o[3] = MFMA32(vf[7], PF(1), o[3]); asm volatile("" : "+v"(o[3])); if (!(H)) KPIECE((u) + 2, 3); FENCE; \
        if (QRL) asm volatile("s_waitcnt vmcnt(4)" : "+v"(qf[0]), "+v"(qf[1]), "+v"(qf[2]), "+v"(qf[3]), "+v"(qf[4]), "+v"(qf[5]), "+v"(qf[6]), "+v"(qf[7]) :: "memory"); \
        mlast = ma; \
        if (DEC) { if (__any(ma > THR)) { \
            const float dl = fmaxf(swap32max(ma), 0.f), f = __builtin_amdgcn_exp2f(-dl); \
            mref += dl; l *= f; B -= dl; negm -= dl; \
            _Pragma("unroll") for (int d = 0; d < 4; ++d) o[d] *= f; \
        } } \
    } while (0)
    float mlast;
    int u = 0;
    for (; u < NA - 1; ++u) {
        HSTEP(sA, sB, u, 0, false, true);
        HSTEP(sB, sA, u, 1, false, true);
    }
    HSTEP(sA, sB, u, 0, true, true);
    negm = splat16(0.f); asm volatile("" : "+v"(negm));
    HSTEP(sB, sA, u, 1, false, false);
    attn_b_store(ws, o, l, (15 - j) * 128 + 32 * wq + r32, head, hh);
    o[0] = splat16(0.f); o[1] = o[0]; o[2] = o[0]; o[3] = o[0]; l = 0.f;
    mref = swap32max(mlast);
    sA -= mref; negm = splat16(-mref); asm volatile("" : "+v"(negm));
    for (++u; u < 33; ++u) {
        HSTEP(sA, sB, u, 0, false, true);
        HSTEP(sB, sA, u, 1, false, true);
    }
    HSTEP(sA, sB, 33, 0, false, true);
    {
#pragma unroll
        for (int i = 0; i < 8; ++i) vf[i] = VFR(33, 1, i);
#pragma unroll
        for (int e = 0; e < 8; e += 2) SM2(sB, e, pw0, e >> 1);
#pragma unroll
        for (int e = 8; e < 16; e += 2) SM2(sB, e, pw1, (e - 8) >> 1);
#pragma unroll
        for (int d = 0; d < 4; ++d) o[d] = MFMA32(vf[d], PF(0), o[d]);
#pragma unroll
        for (int d = 0; d < 4; ++d) o[d] = MFMA32(vf[4 + d], PF(1), o[d]);
        attn_b_store(ws, o, l, j * 128 + 32 * wq + r32, head, hh);
        WAIT_V(0);
    }
#undef KT
#undef KPIECE
#undef VPIECE
#undef KFR
#undef VFR
#undef EXPL
#undef SM2
#undef PF
#undef MX
#undef HSTEP
#undef EX
#undef AD
#undef PK
#undef PINL
#undef FENCE
}

DI void attn_a(ldsp lds, char* ws, const char* xA, const char* wqA, int rt, int hp) {
    const int tid = threadIdx.x, wid = rfl(tid >> 6), lane = tid & 63, r32 = lane & 31, hh = lane >> 5;
    bf16x8 qall[2][2];
    {
        const char* gA = xA + (size_t)rt * 128 * 2048;
        const char* gW = wqA + (size_t)hp * 128;
        const unsigned vA = (unsigned)(tid >> 3) * 2048u + (unsigned)(((tid & 7) ^ ((tid >> 4) & 7)) << 4);
        const unsigned vW = (unsigned)(wid * 8 + ((lane >> 2) & 7)) * 2048u + (unsigned)((lane >> 5) * 64 + (lane & 3) * 16);
        const unsigned lq = ldsaddr_of(lds) + wid * 1024;
#define QSTAGE(t) do { const int _t = (t) < 16 ? (t) : 15; const unsigned _b = lq + ((t) % 3) * 24576; \
            _Pragma("unroll") for (int _i = 0; _i < 4; ++_i) glds16(gA + _t * 128 + _i * 65536, vA, _b + _i * 4096); \
            glds16(gW + (size_t)_t * 131072, vW, _b + 16384); glds16(gW + (size_t)_t * 131072 + 65536, vW, _b + 16384 + 4096); } while (0)
        const int xrow = wid * 32 + r32, xo = xrow * 128, xs = (xrow >> 1) & 7;
        const int wo = 16384 + hh * 1024 + ((lane >> 2) & 3) * 64 + ((lane >> 4) & 1) * 32 + (lane & 3) * 8;
        f32x16 q0 = splat16(0.f), q1 = q0;
        QSTAGE(0); QSTAGE(1);
        for (int t = 0; t < 16; ++t) {
            asm volatile("s_waitcnt vmcnt(6) lgkmcnt(0)\n\ts_barrier" ::: "memory");
            QSTAGE(t + 2);
            ldsp b = lds + (t % 3) * 24576;
#pragma unroll
            for (int s = 0; s < 4; ++s) {
                const bf16x8 xf = ldsr(b + xo + (((2 * s + hh) ^ xs) << 4));
                q0 = MFMA32(ldstr2(b + wo + s * 2048), xf, q0);
                q1 = MFMA32(ldstr2(b + wo + s * 2048 + 512), xf, q1);
            }
        }
#undef QSTAGE
        q0 *= SCALE_A; q1 *= SCALE_A;
        qall[0][0] = packp(q0, 0); qall[0][1] = packp(q0, 8); qall[1][0] = packp(q1, 0); qall[1][1] = packp(q1, 8);
        asm volatile("s_waitcnt vmcnt(0) lgkmcnt(0)\n\ts_barrier" ::: "memory");
    }
    const char* Kg = ws + WS_KA + (size_t)wid * 2048;
    const char* Vg = ws + WS_VA + (size_t)wid * 2048;
    const unsigned l16 = lane * 16, l0 = ldsaddr_of(lds) + wid * 2048;
    constexpr float SHIFT = 40.f;
#define ASTAGE(c) do { const unsigned _b = l0 + ((c) & 3) * 16384; const size_t _o = (size_t)((c) & 15) * 8192; \
        glds16o<0>(Kg + _o, l16, _b); glds16o<1024>(Kg + _o, l16, _b); glds16o<0>(Vg + _o, l16, _b + 8192); glds16o<1024>(Vg + _o, l16, _b + 8192); } while (0)
#define AKF(i, u) ldsr(lds + l16 + (((i) >> 2) & 3) * 16384 + ((i) & 3) * 2048 + (u) * 1024)
#define AVF(i, u) ldsr(lds + l16 + 8192 + (((i) >> 2) & 3) * 16384 + ((i) & 3) * 2048 + (u) * 1024)
#define FENCE do { asm volatile("" ::: "memory"); __builtin_amdgcn_sched_barrier(0); } while (0)
#define EX2(A, e, PW, w, L) do { A[e] = __builtin_amdgcn_exp2f(A[e]); A[(e) + 1] = __builtin_amdgcn_exp2f(A[(e) + 1]); L += A[e]; L += A[(e) + 1]; PW[w] = cvtpk(A[e], A[(e) + 1]); asm volatile("" : "+v"(PW), "+v"(L)); } while (0)
#define BC(x) __builtin_bit_cast(bf16x8, x)
#define PINV(x) asm volatile("" : "+v"(x))
#define ASTEP(A0, A1, B0, B1, PA0, PA1, PA2, PA3, PB0, PB1, PB2, PB3, i) do { \
        float t0 = 0.f, t1 = 0.f; \
        B0 = MFMA32(kf[0], qall[0][0], ng0); PINV(B0); EX2(A0, 0, PA0, 0, t0);  EX2(A0, 2, PA0, 1, t0);  FENCE; \
        B1 = MFMA32(kf[0], qall[1][0], ng1); PINV(B1); EX2(A0, 4, PA0, 2, t0);  EX2(A0, 6, PA0, 3, t0);  FENCE; \
        B0 = MFMA32(kf[1], qall[0][1], B0);  PINV(B0); EX2(A0, 8, PA1, 0, t0);  EX2(A0, 10, PA1, 1, t0); FENCE; \
        B1 = MFMA32(kf[1], qall[1][1], B1);  PINV(B1); EX2(A0, 12, PA1, 2, t0); EX2(A0, 14, PA1, 3, t0); FENCE; \
        kf[0] = AKF((i) + 2, 0); kf[1] = AKF((i) + 2, 1); \
        o0 = MFMA32(vf[0], BC(PB0), o0);     PINV(o0); EX2(A1, 0, PA2, 0, t1);  EX2(A1, 2, PA2, 1, t1);  FENCE; \
        o1 = MFMA32(vf[0], BC(PB2), o1);     PINV(o1); EX2(A1, 4, PA2, 2, t1);  EX2(A1, 6, PA2, 3, t1);  FENCE; \
        o0 = MFMA32(vf[1], BC(PB1), o0);     PINV(o0); EX2(A1, 8, PA3, 0, t1);  EX2(A1, 10, PA3, 1, t1); FENCE; \
        o1 = MFMA32(vf[1], BC(PB3), o1);     PINV(o1); FENCE; \
        vf[0] = AVF(i, 0); vf[1] = AVF(i, 1);          EX2(A1, 12, PA3, 2, t1); EX2(A1, 14, PA3, 3, t1); FENCE; \
        if (__any(!(fmaxf(t0, t1) < 2.f))) {         \
            const f32x16 z = splat16(0.f); const bf16x8 k0 = AKF(i, 0), k1 = AKF(i, 1); \
            A0 = MFMA32(k0, qall[0][0], z); A1 = MFMA32(k0, qall[1][0], z); A0 = MFMA32(k1, qall[0][1], A0); A1 = MFMA32(k1, qall[1][1], A1); \
            const float mn0 = fmaxf(swap32max(rowmax16(A0)), m0), dl0 = mn0 - m0, f0 = __builtin_amdgcn_exp2f(-dl0); \
            const float mn1 = fmaxf(swap32max(rowmax16(A1)), m1), dl1 = mn1 - m1, f1 = __builtin_amdgcn_exp2f(-dl1); \
            m0 = mn0; o0 *= f0; ls0 *= f0; ng0 -= dl0; B0 -= dl0; A0 += ng0; t0 = 0.f; \
            m1 = mn1; o1 *= f1; ls1 *= f1; ng1 -= dl1; B1 -= dl1; A1 += ng1; t1 = 0.f; \
            EX2(A0, 0, PA0, 0, t0); EX2(A0, 2, PA0, 1, t0); EX2(A0, 4, PA0, 2, t0); EX2(A0, 6, PA0, 3, t0); EX2(A0, 8, PA1, 0, t0); EX2(A0, 10, PA1, 1, t0); EX2(A0, 12, PA1, 2, t0); EX2(A0, 14, PA1, 3, t0); \
            EX2(A1, 0, PA2, 0, t1); EX2(A1, 2, PA2, 1, t1); EX2(A1, 4, PA2, 2, t1); EX2(A1, 6, PA2, 3, t1); EX2(A1, 8, PA3, 0, t1); EX2(A1, 10, PA3, 1, t1); EX2(A1, 12, PA3, 2, t1); EX2(A1, 14, PA3, 3, t1); \
        } \
        ls0 += t0; ls1 += t1; \
    } while (0)
    ASTAGE(0); ASTAGE(1);
    asm volatile("s_waitcnt vmcnt(0) lgkmcnt(0)\n\ts_barrier" ::: "memory");
    f32x16 o0 = splat16(0.f), o1 = o0, ng0, ng1, sA0, sA1, sB0, sB1;
    bf16x8 kf[2], vf[2];
    u32x4 pa0, pa1, pa2, pa3, pb0, pb1, pb2, pb3;
    float m0, m1, ls0 = 0.f, ls1 = 0.f;
    {
        kf[0] = AKF(0, 0); kf[1] = AKF(0, 1);
        sA0 = MFMA32(kf[0], qall[0][0], o0); sA1 = MFMA32(kf[0], qall[1][0], o0); sA0 = MFMA32(kf[1], qall[0][1], sA0); sA1 = MFMA32(kf[1], qall[1][1], sA1);
        kf[0] = AKF(1, 0); kf[1] = AKF(1, 1);
        m0 = swap32max(rowmax16(sA0)); m1 = swap32max(rowmax16(sA1));
        ng0 = splat16(-(m0 + SHIFT)); ng1 = splat16(-(m1 + SHIFT)); asm volatile("" : "+v"(ng0), "+v"(ng1));
        sA0 += ng0; sA1 += ng1;
        pb0 = (u32x4){0, 0, 0, 0}; pb1 = pb0; pb2 = pb0; pb3 = pb0;
        vf[0] = __builtin_bit_cast(bf16x8, pb0); vf[1] = vf[0];
    }
    for (int c = 0; c < 16; ++c) {
        if (c == APRIO_OFF) __builtin_amdgcn_s_setprio(0);
        asm volatile("s_waitcnt vmcnt(0) lgkmcnt(0)\n\ts_barrier" ::: "memory");
        ASTAGE(c + 2);
        FENCE;
        ASTEP(sA0, sA1, sB0, sB1, pa0, pa1, pa2, pa3, pb0, pb1, pb2, pb3, 4 * c);
        ASTEP(sB0, sB1, sA0, sA1, pb0, pb1, pb2, pb3, pa0, pa1, pa2, pa3, 4 * c + 1);
        ASTEP(sA0, sA1, sB0, sB1, pa0, pa1, pa2, pa3, pb0, pb1, pb2, pb3, 4 * c + 2);
        ASTEP(sB0, sB1, sA0, sA1, pb0, pb1, pb2, pb3, pa0, pa1, pa2, pa3, 4 * c + 3);
    }
    o0 = MFMA32(vf[0], BC(pb0), o0); o1 = MFMA32(vf[0], BC(pb2), o1); o0 = MFMA32(vf[1], BC(pb1), o0); o1 = MFMA32(vf[1], BC(pb3), o1);
    const int row = rt * 128 + wid * 32 + r32;
    char* orow = ws + WS_OA + (size_t)((row >> 7) * 16 + hp) * 16384 + (size_t)(row & 127) * 128;
#pragma unroll
    for (int hd = 0; hd < 2; ++hd) {
        const f32x16& o = hd ? o1 : o0;
        const float inv = 1.f / swap32sum(hd ? ls1 : ls0);
        unsigned w[8];
#pragma unroll
        for (int i = 0; i < 4; ++i) { w[2 * i] = cvtpk(o[4 * i] * inv, o[4 * i + 1] * inv); w[2 * i + 1] = cvtpk(o[4 * i + 2] * inv, o[4 * i + 3] * inv); }
#pragma unroll
        for (int k = 0; k < 4; k += 2) {
            const auto x0 = __builtin_amdgcn_permlane32_swap(w[2 * k], w[2 * k + 2], false, false);
            const auto x1 = __builtin_amdgcn_permlane32_swap(w[2 * k + 1], w[2 * k + 3], false, false);
            *(u32x4*)(orow + (((hd * 4 + k + hh) ^ (row & 7)) << 4)) = (u32x4){x0[0], x1[0], x0[1], x1[1]};
        }
    }
    WAIT_V(0);
#undef ASTAGE
#undef AKF
#undef AVF
#undef FENCE
#undef EX2
#undef BC
#undef PINV
#undef ASTEP
}

__global__ __launch_bounds__(256, 2) void k_attn(char* __restrict__ ws, const char* __restrict__ woA, const char* __restrict__ woB, const char* __restrict__ xA, const char* __restrict__ wqA) {
    extern __shared__ __attribute__((aligned(16))) unsigned char smem[];
    ldsp lds = (ldsp)smem;
    const int b = blockIdx.x & 255, xcd = b & 7, idx = b >> 3;
    if (blockIdx.x < 256) {
        attn_b(lds, ws, xcd * 4 + (idx >> 3), idx & 7);
    } else {
        { const int wid = rfl(threadIdx.x >> 6); const unsigned l16 = (threadIdx.x & 63) * 16;
          for (int i = 0; i < 8; ++i) glds16(woB + (size_t)b * 32768 + i * 4096 + wid * 1024, l16, ldsaddr_of(lds) + 73728 + wid * 1024);
          for (int i = 0; i < 2; ++i) glds16(woA + (size_t)b * 8192 + i * 4096 + wid * 1024, l16, ldsaddr_of(lds) + 73728 + wid * 1024); }
        __builtin_amdgcn_s_setprio(1);
        attn_a(lds, ws, xA, wqA, 2 * xcd + (idx >> 4), idx & 15);
    }
}

constexpr int SPLIT0 = 42;
DI void store_out(char* outp  , const f32x4 (&acc)[4][2]) {
    const int tid = threadIdx.x, wid = rfl(tid >> 6), lane = tid & 63, wr = wid >> 2, wc = wid & 3, fr = lane & 15, fq = lane >> 4;
    const int c8 = (fq >> 1) | ((fq & 1) << 1);
#pragma unroll
    for (int m = 0; m < 4; ++m) {
        const f32x4 v0 = acc[m][0], v1 = acc[m][1];
        const auto r0 = __builtin_amdgcn_permlane16_swap(cvtpk(v0[0], v0[1]), cvtpk(v1[0], v1[1]), false, false);
        const auto r1 = __builtin_amdgcn_permlane16_swap(cvtpk(v0[2], v0[3]), cvtpk(v1[2], v1[3]), false, false);
        *(u32x4*)(outp + (size_t)(wr * 64 + m * 16 + fr) * 4096 + (size_t)(wc * 64 + c8 * 16)) = (u32x4){r0[0], r1[0], r0[1], r1[1]};
    }
}

template <int ROLE>
DI void oproj(ldsp lds, const char* ctx, const char* woB, const char* oa, const char* woA, char* part, unsigned* flag, char* outp) {
    const int tid = threadIdx.x, wid = rfl(tid >> 6), lane = tid & 63, wr = wid >> 2, wc = wid & 3, fr = lane & 15, fq = lane >> 4;
    constexpr int N1 = ROLE ? 64 - SPLIT0 : SPLIT0, NS = ROLE ? N1 + 16 : N1;
    unsigned voffA[2], voffW[2];
#pragma unroll
    for (int i = 0; i < 2; ++i) {
        voffA[i] = (unsigned)(tid * 16 + i * 8192);
        const int kg = (tid >> 7) + 4 * i, ng = (tid >> 5) & 3, r = (tid >> 2) & 7, c = (tid & 3) ^ (((tid >> 7) & 1) << 1);
        voffW[i] = (unsigned)(kg * 8 + r) * 2048u + (unsigned)(ng * 64 + c * 16);
    }
    const int aoff0 = (wr * 64 + fr) * 128 + ((fq ^ (fr & 7)) << 4), aoff1 = aoff0 ^ 64;
    const int boff0 = 16384 + (fq * 4 + wc) * 512 + ((lane >> 2) & 3) * 64 + (((lane & 3) * 8) ^ ((fq & 1) << 5)), boff1 = boff0 ^ 32;
    const __amdgpu_buffer_rsrc_t rs = __builtin_amdgcn_make_buffer_rsrc(part, 0, 65536, 0x00020000);
#define MPIECE(t, i) do { const int _t = (t) < NS ? (t) : NS - 1; const unsigned _b = ldsaddr_of(lds) + ((t) & 3) * 32768 + wid * 1024; \
        if (!ROLE || _t < N1) { \
            if ((i) < 2) glds16(ctx + (size_t)_t * 16384, voffA[(i) & 1], _b + ((i) & 1) * 8192); \
            else glds16(woB + (size_t)_t * 131072, voffW[(i) & 1], _b + 16384 + ((i) & 1) * 8192); \
        } else { \
            if ((i) < 2) glds16(oa + (size_t)(_t - N1) * 16384, voffA[(i) & 1], _b + ((i) & 1) * 8192); \
            else glds16(woA + (size_t)(_t - N1) * 131072, voffW[(i) & 1], _b + 16384 + ((i) & 1) * 8192); \
        } } while (0)
#define MFENCE do { asm volatile("" ::: "memory"); __builtin_amdgcn_sched_barrier(0); } while (0)
#define MRDA(AF, t, m) do { ldsp _b = lds + ((t) & 3) * 32768; AF[m][0] = ldsr(_b + aoff0 + (m) * 2048); AF[m][1] = ldsr(_b + aoff1 + (m) * 2048); } while (0)
#define MRDB(BF, t, k) do { ldsp _b = lds + ((t) & 3) * 32768; BF[0][k] = ldstr2(_b + boff0 + (k) * 8192); BF[1][k] = ldstr2(_b + boff1 + (k) * 8192); } while (0)
#define MM4(AF, BF, k, m0) do { acc[m0][0] = MFMA16(BF[0][k], AF[m0][k], acc[m0][0]); acc[m0][1] = MFMA16(BF[1][k], AF[m0][k], acc[m0][1]); \
        acc[(m0) + 1][0] = MFMA16(BF[0][k], AF[(m0) + 1][k], acc[(m0) + 1][0]); acc[(m0) + 1][1] = MFMA16(BF[1][k], AF[(m0) + 1][k], acc[(m0) + 1][1]); } while (0)
#define MSTEPX(AX, BX, AY, BY, t, VM, LD) do { \
        asm volatile("s_waitcnt vmcnt(" #VM ") lgkmcnt(0)\n\ts_barrier" ::: "memory"); MFENCE; \
        MRDB(BY, (t) + 1, 0); MRDA(AY, (t) + 1, 0); MM4(AX, BX, 0, 0); if (LD) MPIECE((t) + 4, 0); MFENCE; \
        MRDA(AY, (t) + 1, 1); MRDA(AY, (t) + 1, 2); MM4(AX, BX, 0, 2); if (LD) MPIECE((t) + 4, 1); MFENCE; \
        MRDB(BY, (t) + 1, 1); MRDA(AY, (t) + 1, 3); MM4(AX, BX, 1, 0); if (LD) MPIECE((t) + 4, 2); MFENCE; \
        MM4(AX, BX, 1, 2); if (LD) MPIECE((t) + 4, 3); MFENCE; \
    } while (0)
#define MSTEP(AX, BX, AY, BY, t) MSTEPX(AX, BX, AY, BY, t, 8, true)
    f32x4 acc[4][2];
#pragma unroll
    for (int m = 0; m < 4; ++m)
#pragma unroll
        for (int n = 0; n < 2; ++n) acc[m][n] = (f32x4){0.f, 0.f, 0.f, 0.f};
    bf16x8 A0[4][2], B0[2][2], A1[4][2], B1[2][2];
#pragma unroll
    for (int i = 0; i < 4; ++i) MPIECE(0, i);
#pragma unroll
    for (int i = 0; i < 4; ++i) MPIECE(1, i);
#pragma unroll
    for (int i = 0; i < 4; ++i) MPIECE(2, i);
#pragma unroll
    for (int i = 0; i < 4; ++i) MPIECE(3, i);
    WAIT_V(12); BARF;
    MRDB(B0, 0, 0); MRDB(B0, 0, 1); MRDA(A0, 0, 0); MRDA(A0, 0, 1); MRDA(A0, 0, 2); MRDA(A0, 0, 3);
    if constexpr (ROLE == 1) {
        for (int t = 0; t < N1; t += 2) { MSTEP(A0, B0, A1, B1, t); MSTEP(A1, B1, A0, B0, t + 1); }
        f32x4 hold[4][2];
#pragma unroll
        for (int m = 0; m < 4; ++m)
#pragma unroll
            for (int n = 0; n < 2; ++n) { hold[m][n] = acc[m][n]; acc[m][n] = (f32x4){0.f, 0.f, 0.f, 0.f}; }
#define PUB(m) do { __builtin_amdgcn_raw_buffer_store_b128(__builtin_bit_cast(u32x4, hold[m][0]), rs, ((m) * 2) * 8192 + tid * 16, 0, 16); \
                    __builtin_amdgcn_raw_buffer_store_b128(__builtin_bit_cast(u32x4, hold[m][1]), rs, ((m) * 2 + 1) * 8192 + tid * 16, 0, 16); MFENCE; } while (0)
        PUB(0); MSTEPX(A0, B0, A1, B1, N1, 10, true);     PUB(1); MSTEPX(A1, B1, A0, B0, N1 + 1, 12, true);
        PUB(2); MSTEPX(A0, B0, A1, B1, N1 + 2, 14, true); PUB(3); MSTEPX(A1, B1, A0, B0, N1 + 3, 14, true);
#undef PUB
        MSTEPX(A0, B0, A1, B1, N1 + 4, 12, true); MSTEPX(A1, B1, A0, B0, N1 + 5, 10, true);
        MSTEP(A0, B0, A1, B1, N1 + 6);
        if (tid == 0) __hip_atomic_store(flag, 1u, __ATOMIC_RELAXED, __HIP_MEMORY_SCOPE_AGENT);
        MFENCE;
        MSTEP(A1, B1, A0, B0, N1 + 7);
        for (int t = N1 + 8; t < NS - 4; t += 2) { MSTEP(A0, B0, A1, B1, t); MSTEP(A1, B1, A0, B0, t + 1); }
        MSTEPX(A0, B0, A1, B1, NS - 4, 8, false); MSTEPX(A1, B1, A0, B0, NS - 3, 4, false);
        MSTEPX(A0, B0, A1, B1, NS - 2, 0, false); MSTEPX(A1, B1, A0, B0, NS - 1, 0, false);
        store_out(outp, acc);
    } else {
        for (int t = 0; t < NS - 6; t += 2) { MSTEP(A0, B0, A1, B1, t); MSTEP(A1, B1, A0, B0, t + 1); }
        MSTEP(A0, B0, A1, B1, NS - 6);
        if (wid == 0) { while (rfl((int)__hip_atomic_load(flag, __ATOMIC_RELAXED, __HIP_MEMORY_SCOPE_AGENT)) != 1) __builtin_amdgcn_s_sleep(2); }
        MFENCE;
        MSTEP(A1, B1, A0, B0, NS - 5);
        u32x4 pp[4][2];
#pragma unroll
        for (int m = 0; m < 4; ++m)
#pragma unroll
            for (int n = 0; n < 2; ++n) pp[m][n] = __builtin_amdgcn_raw_buffer_load_b128(rs, (m * 2 + n) * 8192 + tid * 16, 0, 16);
        MFENCE;
        MSTEPX(A0, B0, A1, B1, NS - 4, 16, false); MSTEPX(A1, B1, A0, B0, NS - 3, 12, false);
        MSTEPX(A0, B0, A1, B1, NS - 2, 8, false);  MSTEPX(A1, B1, A0, B0, NS - 1, 8, false);
#pragma unroll
        for (int m = 0; m < 4; ++m)
#pragma unroll
            for (int n = 0; n < 2; ++n) acc[m][n] += __builtin_bit_cast(f32x4, pp[m][n]);
        store_out(outp, acc);
    }
    WAIT_V(0);
#undef MPIECE
#undef MFENCE
#undef MRDA
#undef MRDB
#undef MM4
#undef MSTEP
#undef MSTEPX
}

__global__ __launch_bounds__(512, 2) void k_oproj(const char* __restrict__ woA, const char* __restrict__ woB, char* __restrict__ ws, char* __restrict__ out) {
    extern __shared__ __attribute__((aligned(16))) unsigned char smem[];
    ldsp lds = (ldsp)smem;
    const int b = blockIdx.x, xcd = b & 7, idx = b >> 3, role = xcd >> 2;
    const int rt = 4 * (xcd & 3) + (idx & 3), ct = idx >> 2, tile = rt * 8 + ct;
    const char* ctx = ws + WS_CTX + (size_t)rt * 128 * 8192;
    unsigned* flag = (unsigned*)(ws + WS_FLAG + tile * 128);
    char* part = ws + WS_PART + (size_t)tile * 65536;
    if (role == 1) oproj<1>(lds, ctx + SPLIT0 * 16384, woB + (size_t)SPLIT0 * 131072 + ct * 256, ws + WS_OA + (size_t)rt * 128 * 2048, woA + ct * 256, part, flag, out + (size_t)rt * 128 * 4096 + ct * 256);
    else oproj<0>(lds, ctx, woB + ct * 256, nullptr, nullptr, part, flag, out + (size_t)rt * 128 * 4096 + 2048 + ct * 256);
}

extern "C" void kernel_launch(void* const* d_in, const int* in_sizes, int n_in, void* d_out, int out_size, void* d_ws, size_t ws_size, hipStream_t stream) {
    (void)in_sizes; (void)n_in; (void)out_size; (void)ws_size;
    constexpr int LDS1 = 163840, LDS2 = 73728 + 4096, LDS3 = 131072;
    (void)hipFuncSetAttribute((const void*)k_proj, hipFuncAttributeMaxDynamicSharedMemorySize, LDS1);
    (void)hipFuncSetAttribute((const void*)k_attn, hipFuncAttributeMaxDynamicSharedMemorySize, LDS2);
    (void)hipFuncSetAttribute((const void*)k_oproj, hipFuncAttributeMaxDynamicSharedMemorySize, LDS3);
    char* ws = (char*)d_ws;
    k_proj<<<256, 512, LDS1, stream>>>((const char*)d_in[0], (const char*)d_in[1], (const char*)d_in[2], (const char*)d_in[3], (const char*)d_in[5],
                                       (const char*)d_in[6], (const char*)d_in[7], (const char*)d_in[8], (const char*)d_in[9], ws);
    k_attn<<<512, 256, LDS2, stream>>>(ws, (const char*)d_in[4], (const char*)d_in[10], (const char*)d_in[0], (const char*)d_in[1]);
    k_oproj<<<256, 512, LDS3, stream>>>((const char*)d_in[4], (const char*)d_in[10], ws, (char*)d_out);
}
```

```cpp
#include <hip/hip_runtime.h>
#include <stdint.h>

#define LAS __attribute__((address_space(3)))
#define DI __device__ __forceinline__
typedef unsigned short u16;
typedef short bf16x8 __attribute__((ext_vector_type(8)));
typedef short s16x4 __attribute__((ext_vector_type(4)));
typedef float f32x4 __attribute__((ext_vector_type(4)));
typedef float f32x16 __attribute__((ext_vector_type(16)));
typedef unsigned u32x4 __attribute__((ext_vector_type(4)));
typedef unsigned u32x2 __attribute__((ext_vector_type(2)));
typedef LAS unsigned char* ldsp;

constexpr size_t WS_QB = 0;
constexpr size_t WS_KB = WS_QB + (16u << 20);
constexpr size_t WS_VB = WS_KB + (16u << 20);
constexpr size_t WS_KA = WS_VB + (16u << 20);
constexpr size_t WS_VA = WS_KA + (128u << 10);
constexpr size_t WS_CTX = WS_VA + (128u << 10);
constexpr size_t WS_OA = WS_CTX + (16u << 20);
constexpr size_t WS_PART = WS_OA + (4u << 20);
constexpr size_t WS_FLAG = WS_PART + (8u << 20);

constexpr float LOG2E = 1.4426950408889634f;
constexpr float SCALE_A = 0.17677669529663687f * LOG2E;
constexpr float SCALE_B = 0.08838834764831845f * LOG2E;
constexpr float THR = 40.f;

#define WAIT_V(n) asm volatile("s_waitcnt vmcnt(" #n ")" ::: "memory")
#define WAIT_L(n) asm volatile("s_waitcnt lgkmcnt(" #n ")" ::: "memory")
#define BAR __builtin_amdgcn_s_barrier()
#define BARF do { asm volatile("" ::: "memory"); __builtin_amdgcn_s_barrier(); asm volatile("" ::: "memory"); } while (0)
#define SCHED __builtin_amdgcn_sched_barrier(0)
#define MFMA16(a, b, c) __builtin_amdgcn_mfma_f32_16x16x32_bf16(a, b, c, 0, 0, 0)
#define MFMA32(a, b, c) __builtin_amdgcn_mfma_f32_32x32x16_bf16(a, b, c, 0, 0, 0)

typedef float f32x2 __attribute__((ext_vector_type(2)));
typedef __bf16 bf16x2_t __attribute__((ext_vector_type(2)));
DI unsigned cvtpk(float lo, float hi) { const f32x2 v = {lo, hi}; return __builtin_bit_cast(unsigned, __builtin_convertvector(v, bf16x2_t)); }
DI void glds16(const char* sbase, unsigned voff, unsigned ldsaddr) {
    asm volatile("s_mov_b32 m0, %2\n\ts_nop 0\n\tglobal_load_lds_dwordx4 %0, %1" :: "v"(voff), "s"(sbase), "s"(ldsaddr) : "memory");
}
template <int OFF> DI void glds16o(const char* sbase, unsigned voff, unsigned ldsaddr) {
    asm volatile("s_mov_b32 m0, %2\n\ts_nop 0\n\tglobal_load_lds_dwordx4 %0, %1 offset:%3" :: "v"(voff), "s"(sbase), "s"(ldsaddr), "i"(OFF) : "memory");
}
DI unsigned ldsaddr_of(ldsp p) { return (unsigned)(size_t)p; }
DI bf16x8 ldsr(ldsp p) { return *(const LAS bf16x8*)p; }
DI s16x4 ldstr(ldsp p) { return __builtin_amdgcn_ds_read_tr16_b64_v4i16((LAS s16x4*)p); }
DI bf16x8 ldstr2(ldsp p) { const s16x4 a = ldstr(p), b = ldstr(p + 256); return (bf16x8){a[0], a[1], a[2], a[3], b[0], b[1], b[2], b[3]}; }
DI int rfl(int x) { return __builtin_amdgcn_readfirstlane(x); }

DI void warm(const char* base, int npieces, unsigned dump) {
    const int wid = rfl(threadIdx.x >> 6); const unsigned l16 = (threadIdx.x & 63) * 16;
    for (int i = 0; i < npieces; ++i) glds16(base + (size_t)i * 8192 + wid * 1024, l16, dump + wid * 1024);
}

constexpr int HTB = 16384;
#define SA(b, h) (((b) * 2 + (h)) * HTB)
#define SB(b, h) ((4 + (b) * 2 + (h)) * HTB)

template <bool SWAP, bool KVT>
DI void gemm_big(ldsp lds, const char* gA, unsigned lda2, const char* gW, unsigned ldw2, int nt, int nunits,
                 char* out, int row0, int head0, float scale, const char* wk, const char* wv, const char* xkv) {
    const int tid = threadIdx.x, wid = rfl(tid >> 6), lane = tid & 63, wr = wid >> 2, wc = wid & 3, fr = lane & 15, fq = lane >> 4;
    unsigned voffA[2], voffW[2];
#pragma unroll
    for (int i = 0; i < 2; ++i) {
        voffA[i] = (unsigned)((tid >> 3) + 64 * i) * lda2 + (unsigned)(((tid & 7) ^ ((tid >> 3) & 7)) << 4);
        const int kg = (tid >> 7) + 4 * i, ng = (tid >> 5) & 3, r = (tid >> 2) & 7, c = (tid & 3) ^ (((tid >> 7) & 1) << 1);
        voffW[i] = (unsigned)(kg * 8 + r) * ldw2 + (unsigned)(ng * 64 + c * 16);
    }
    const size_t kstepA = 128, kstepW = (size_t)64 * ldw2, hstepA = (size_t)128 * lda2, hstepW = 256;
    const unsigned ldsw = ldsaddr_of(lds) + (unsigned)wid * 1024u;
    const int rowb = wr * 64 + (SWAP ? 16 * (fr >> 3) + (fr & 7) : fr);
    const int aoff0 = rowb * 128 + ((fq ^ (fr & 7)) << 4), aoff1 = aoff0 ^ 64;
    const int boff0 = (fq * 4 + wc) * 512 + ((lane >> 2) & 3) * 64 + (((lane & 3) * 8) ^ ((fq & 1) << 5)), boff1 = boff0 ^ 32;
#define MOFF(m) (SWAP ? ((m) >> 1) * 4096 + ((m) & 1) * 1024 : (m) * 2048)
#define STAGE(bufoff, gbase, voff) do { _Pragma("unroll") for (int _i = 0; _i < 2; ++_i) glds16((gbase), (voff)[_i], (bufoff) + ldsw + _i * 8192); } while (0)
    const unsigned voffkv = (unsigned)((lane >> 2) * 64 + (((lane & 3) ^ (((lane >> 5) & 1) << 1)) << 4));
#define KVSTAGE(bufoff) do { const char* _s = (((bufoff) >> 16) ? wv : wk) + (size_t)wid * 8192 + (((bufoff) >> 14) & 3) * 2048; \
        glds16(_s, voffkv, (bufoff) + ldsw); glds16(_s + 1024, voffkv, (bufoff) + ldsw + 8192); } while (0)
#define STAGE2(KVL, bufoff, gbase, voff) do { if (KVL) KVSTAGE(bufoff); else STAGE(bufoff, gbase, voff); } while (0)
#define LDA(dst, b, h) do { _Pragma("unroll") for (int m = 0; m < 4; ++m) { dst[m][0] = ldsr(lds + SA(b, h) + aoff0 + MOFF(m)); dst[m][1] = ldsr(lds + SA(b, h) + aoff1 + MOFF(m)); } } while (0)
#define LDB(dst, b, h) do { _Pragma("unroll") for (int k = 0; k < 2; ++k) { dst[0][k] = ldstr2(lds + SB(b, h) + boff0 + k * 8192); dst[1][k] = ldstr2(lds + SB(b, h) + boff1 + k * 8192); } } while (0)
#define MMA(ai, bj, At, Bt) do { __builtin_amdgcn_s_setprio(1); _Pragma("unroll") for (int m = 0; m < 4; ++m) _Pragma("unroll") for (int n = 0; n < 2; ++n) _Pragma("unroll") for (int k = 0; k < 2; ++k) \
        acc[ai][bj][m][n] = SWAP ? MFMA16(At[m][k], Bt[n][k], acc[ai][bj][m][n]) : MFMA16(Bt[n][k], At[m][k], acc[ai][bj][m][n]); __builtin_amdgcn_s_setprio(0); } while (0)
    f32x4 acc[2][2][4][2];
#pragma unroll
    for (int a = 0; a < 2; ++a)
#pragma unroll
        for (int b = 0; b < 2; ++b)
#pragma unroll
            for (int m = 0; m < 4; ++m)
#pragma unroll
                for (int n = 0; n < 2; ++n) acc[a][b][m][n] = (f32x4){0.f, 0.f, 0.f, 0.f};
    bf16x8 At[4][2], B0[2][2], B1[2][2];
    const char* cA = gA; const char* cW = gW;
    STAGE(SB(0, 0), cW, voffW); STAGE(SA(0, 0), cA, voffA); STAGE(SB(0, 1), cW + hstepW, voffW); STAGE(SA(0, 1), cA + hstepA, voffA);
    if (wr == 1) BAR;
    WAIT_V(4); BAR;
    STAGE(SB(1, 0), cW + kstepW, voffW); STAGE(SA(1, 0), cA + kstepA, voffA); STAGE(SB(1, 1), cW + hstepW + kstepW, voffW);
    WAIT_V(6); BAR;
    for (int ui = 0;; ++ui) {
        const bool has_next = ui + 1 < nunits;
        const char* nW = has_next ? cW + 512 : cW;
#define KBODY(t, KVL) do { \
            const bool last = (t == nt - 2); \
            const char* a1 = cA + (size_t)(t + 1) * kstepA; \
            const char* a2 = last ? cA : cA + (size_t)(t + 2) * kstepA; const char* b2 = last ? nW : cW + (size_t)(t + 2) * kstepW; \
            const char* a3 = a2 + kstepA; const char* b3 = b2 + kstepW; \
            const bool fresh = (ui > 0 && t == 0); \
            LDB(B0, 0, 0); SCHED; LDA(At, 0, 0); if (!fresh) STAGE(SA(1, 1), a1 + hstepA, voffA); \
            WAIT_L(8); BAR; WAIT_L(0); MMA(0, 0, At, B0); BAR; SCHED; \
            LDB(B1, 0, 1); STAGE2(KVL, SB(0, 0), b2, voffW); \
            BAR; WAIT_L(0); MMA(0, 1, At, B1); BAR; \
            LDA(At, 0, 1); STAGE2(KVL, SA(0, 0), a2, voffA); \
            BAR; WAIT_L(0); MMA(1, 0, At, B0); BAR; SCHED; \
            STAGE2(KVL, SB(0, 1), b2 + hstepW, voffW); \
            if (fresh) WAIT_V(22); else WAIT_V(6); \
            BAR; MMA(1, 1, At, B1); BAR; \
            LDB(B0, 1, 0); SCHED; LDA(At, 1, 0); STAGE2(KVL, SA(0, 1), a2 + hstepA, voffA); \
            WAIT_L(8); BAR; WAIT_L(0); MMA(0, 0, At, B0); BAR; SCHED; \
            LDB(B1, 1, 1); STAGE2(KVL, SB(1, 0), b3, voffW); \
            BAR; WAIT_L(0); MMA(0, 1, At, B1); BAR; \
            LDA(At, 1, 1); STAGE2(KVL, SA(1, 0), a3, voffA); \
            BAR; WAIT_L(0); MMA(1, 0, At, B0); BAR; SCHED; \
            STAGE2(KVL, SB(1, 1), b3 + hstepW, voffW); \
            WAIT_V(6); BAR; MMA(1, 1, At, B1); BAR; \
        } while (0)
        for (int t = 0; t < (KVT ? nt - 2 : nt); t += 2) KBODY(t, false);
        if constexpr (KVT) KBODY(nt - 2, true);
        if (has_next) STAGE(SA(1, 1), cA + kstepA + hstepA, voffA);
        if (KVT) { KVSTAGE(SA(1, 1));
            const unsigned vx = (unsigned)(fr * 2048 + fq * 16);
#pragma unroll
            for (int k = 0; k < 4; ++k) glds16(xkv + wid * 256 + k * 64, vx, ldsaddr_of(lds) + 131072 + wid * 4096 + k * 1024); }
        const int h0 = head0 + 2 * ui;
        int tz = threadIdx.x; asm volatile("" : "+v"(tz));
        const int fr = tz & 15, fq = (tz >> 4) & 3;
        if constexpr (!SWAP) {
#pragma unroll
            for (int ai = 0; ai < 2; ++ai)
#pragma unroll
                for (int m = 0; m < 4; ++m) {
                    const int t = row0 + ai * 128 + wr * 64 + m * 16 + fr;
#pragma unroll
                    for (int bj = 0; bj < 2; ++bj) {
                        const f32x4 v0 = acc[ai][bj][m][0] * scale, v1 = acc[ai][bj][m][1] * scale;
                        const auto r0 = __builtin_amdgcn_permlane16_swap(cvtpk(v0[0], v0[1]), cvtpk(v1[0], v1[1]), false, false);
                        const auto r1 = __builtin_amdgcn_permlane16_swap(cvtpk(v0[2], v0[3]), cvtpk(v1[2], v1[3]), false, false);
                        const u32x4 w = {r0[0], r1[0], r0[1], r1[1]};
                        const size_t off = ((size_t)(((h0 + bj) * 64 + (t >> 5)) * 8 + 2 * wc + (fq & 1))) * 1024 + (size_t)(((t & 31) + 32 * (fq >> 1)) * 16);
                        *(u32x4*)(out + off) = w;
                    }
                }
        } else {
#pragma unroll
            for (int ai = 0; ai < 2; ++ai)
#pragma unroll
                for (int mp = 0; mp < 2; ++mp) {
                    const int pos = row0 + ai * 128 + wr * 64 + 32 * mp + 8 * fq;
#pragma unroll
                    for (int bj = 0; bj < 2; ++bj)
#pragma unroll
                        for (int n = 0; n < 2; ++n) {
                            const f32x4 a = acc[ai][bj][2 * mp][n], b = acc[ai][bj][2 * mp + 1][n];
                            const u32x4 w = {cvtpk(a[0], a[1]), cvtpk(a[2], a[3]), cvtpk(b[0], b[1]), cvtpk(b[2], b[3])};
                            const size_t off = ((size_t)(((h0 + bj) * 128 + (pos >> 4)) * 4 + wc)) * 1024 + (size_t)((n * 16 + fr + 32 * (fq & 1)) * 16);
                            *(u32x4*)(out + off) = w;
                        }
                }
        }
        if (!has_next) break;
#pragma unroll
        for (int a = 0; a < 2; ++a)
#pragma unroll
            for (int b = 0; b < 2; ++b)
#pragma unroll
                for (int m = 0; m < 4; ++m)
#pragma unroll
                    for (int n = 0; n < 2; ++n) acc[a][b][m][n] = (f32x4){0.f, 0.f, 0.f, 0.f};
        cW = nW;
    }
    WAIT_V(16);
    if (wr == 0) BAR;
    BARF;
#undef MOFF
#undef KVSTAGE
#undef KBODY
#undef STAGE2
#undef STAGE
#undef LDA
#undef LDB
#undef MMA
}

DI void gemm_kv(ldsp lds, const char* xA, const char* wk, const char* wv, int r16, char* ws) {
    const int tid = threadIdx.x, wid = rfl(tid >> 6), lane = tid & 63, fr = lane & 15, fq = lane >> 4;
    bf16x8 xf[4];
#pragma unroll
    for (int k = 0; k < 4; ++k) xf[k] = ldsr(lds + 131072 + wid * 4096 + k * 1024 + lane * 16);
    ldsp wb = lds + wid * 1024 + (fq >> 1) * 8192 + (fq & 1) * 512 + ((lane >> 2) & 3) * 64;
    const int c0 = ((lane & 3) * 8) ^ ((fq & 1) << 5);
    f32x4 acc[4];
#pragma unroll
    for (int n = 0; n < 4; ++n) acc[n] = (f32x4){0.f, 0.f, 0.f, 0.f};
#pragma unroll
    for (int k = 0; k < 4; ++k)
#pragma unroll
        for (int n = 0; n < 4; ++n) acc[n] = MFMA16(ldstr2(wb + ((n >> 1) * 4 + k) * 16384 + (c0 ^ ((n & 1) << 5))), xf[k], acc[n]);
    WAIT_L(0); BARF;
#pragma unroll
    for (int n = 0; n < 4; ++n) *(LAS f32x4*)(lds + (n * 8 + wid) * 1024 + lane * 16) = acc[n];
    WAIT_L(0); BARF;
    if (wid < 4) {
        f32x4 v = *(const LAS f32x4*)(lds + (wid * 8) * 1024 + lane * 16);
#pragma unroll
        for (int w = 1; w < 8; ++w) v += *(const LAS f32x4*)(lds + (wid * 8 + w) * 1024 + lane * 16);
        const int t = r16 * 16 + fr;
        const unsigned w0 = cvtpk(v[0], v[1]), w1 = cvtpk(v[2], v[3]);
        if (wid < 2) {
            const size_t off = ((size_t)((t >> 5) * 2 + wid)) * 1024 + (size_t)(((t & 31) + 32 * (fq & 1)) * 16 + (fq >> 1) * 8);
            *(u32x2*)(ws + WS_KA + off) = (u32x2){w0, w1};
        } else {
            const int pos = (t & ~12) | ((t & 4) << 1) | ((t & 8) >> 1);
            const int d = (wid - 2) * 16 + 4 * fq;
            char* p = ws + WS_VA + (size_t)(pos >> 4) * 1024 + (size_t)((d + 32 * ((pos >> 3) & 1)) * 16 + (pos & 7) * 2);
            *(u16*)(p) = (u16)w0; *(u16*)(p + 16) = (u16)(w0 >> 16); *(u16*)(p + 32) = (u16)w1; *(u16*)(p + 48) = (u16)(w1 >> 16);
        }
    }
}

__global__ __launch_bounds__(512, 2) void k_proj(const char* __restrict__ xA, const char* __restrict__ wqA, const char* __restrict__ wkA, const char* __restrict__ wvA,
                                                 const char* __restrict__ xB, const char* __restrict__ ckv, const char* __restrict__ wqB,
                                                 const char* __restrict__ wuk, const char* __restrict__ wuv, char* __restrict__ ws) {
    extern __shared__ __attribute__((aligned(16))) unsigned char smem[];
    ldsp lds = (ldsp)smem;
    const int b = blockIdx.x, xcd = b & 7, idx = b >> 3;
    if (b == 0 && threadIdx.x < 128) *(unsigned*)(ws + WS_FLAG + threadIdx.x * 128) = 0u;
    warm(xA + (size_t)b * 16384, 2, ldsaddr_of(lds) + 131072);
    warm(wqA + (size_t)b * 8192, 1, ldsaddr_of(lds) + 131072);
    if (idx < 16) warm((idx < 8 ? wkA : wvA) + (size_t)(idx & 7) * 8192, 1, ldsaddr_of(lds) + 131072);
    if (idx < 16) {
        const int rt = 4 * (xcd & 1) + (idx & 3), ct = 4 * (xcd >> 1) + (idx >> 2);
        gemm_big<false, true>(lds, xB + (size_t)rt * 256 * 2048, 2048u, wqB + (size_t)ct * 512, 8192u, 16, 1, ws + WS_QB, rt * 256, ct * 2, SCALE_B, wkA, wvA, xA + (size_t)(xcd * 16 + idx) * 16 * 2048);
        gemm_kv(lds, xA, wkA, wvA, xcd * 16 + idx, ws);
    } else if (idx < 24) {
        const int i = idx - 16, rt = 4 * (xcd & 1) + (i & 3), cp = 2 * (xcd >> 1) + (i >> 2);
        gemm_big<false, false>(lds, ckv + (size_t)rt * 256 * 1024, 1024u, wuk + (size_t)cp * 1024, 8192u, 8, 2, ws + WS_KB, rt * 256, cp * 4, 1.f, nullptr, nullptr, nullptr);
    } else {
        const int i = idx - 24, rt = 4 * (xcd & 1) + (i & 3), cp = 2 * (xcd >> 1) + (i >> 2);
        gemm_big<true, false>(lds, ckv + (size_t)rt * 256 * 1024, 1024u, wuv + (size_t)cp * 1024, 8192u, 8, 2, ws + WS_VB, rt * 256, cp * 4, 1.f, nullptr, nullptr, nullptr);
    }
}

DI float rowmax16(const f32x16& p) { float a = fmaxf(p[0], p[1]); _Pragma("unroll") for (int r = 2; r < 16; r += 2) a = fmaxf(fmaxf(a, p[r]), p[r + 1]); return a; }
constexpr int APRIO_OFF = 4;
DI float swap32max(float m) { const auto rr = __builtin_amdgcn_permlane32_swap(__float_as_uint(m), __float_as_uint(m), false, false); return fmaxf(__uint_as_float(rr[0]), __uint_as_float(rr[1])); }
DI float swap32sum(float m) { const auto rr = __builtin_amdgcn_permlane32_swap(__float_as_uint(m), __float_as_uint(m), false, false); return __uint_as_float(rr[0]) + __uint_as_float(rr[1]); }
DI bf16x8 packp(const f32x16& p, int b) { const u32x4 w = {cvtpk(p[b], p[b + 1]), cvtpk(p[b + 2], p[b + 3]), cvtpk(p[b + 4], p[b + 5]), cvtpk(p[b + 6], p[b + 7])}; return __builtin_bit_cast(bf16x8, w); }
DI f32x16 splat16(float v) { f32x16 r; _Pragma("unroll") for (int i = 0; i < 16; ++i) r[i] = v; return r; }

DI void attn_b_store(char* ws, const f32x16 (&o)[4], float l, int row, int head, int hh) {
    const float inv = 1.f / swap32sum(l);
    char* orow = ws + WS_CTX + (size_t)((row >> 7) * 64 + 2 * head) * 16384 + (size_t)(row & 127) * 128;
#pragma unroll
    for (int dd = 0; dd < 4; ++dd) {
        unsigned w[8];
#pragma unroll
        for (int i = 0; i < 4; ++i) { w[2 * i] = cvtpk(o[dd][4 * i] * inv, o[dd][4 * i + 1] * inv); w[2 * i + 1] = cvtpk(o[dd][4 * i + 2] * inv, o[dd][4 * i + 3] * inv); }
#pragma unroll
        for (int k = 0; k < 4; k += 2) {
            const auto x0 = __builtin_amdgcn_permlane32_swap(w[2 * k], w[2 * k + 2], false, false);
            const auto x1 = __builtin_amdgcn_permlane32_swap(w[2 * k + 1], w[2 * k + 3], false, false);
            *(u32x4*)(orow + (dd >> 1) * 16384 + ((((dd & 1) * 4 + k + hh) ^ (row & 7)) << 4)) = (u32x4){x0[0], x1[0], x0[1], x1[1]};
        }
    }
}

DI void attn_b(ldsp lds, char* ws, int head, int j) {
    const int tid = threadIdx.x, wq = rfl(tid >> 6), lane = tid & 63, r32 = lane & 31, hh = lane >> 5;
    const char* Kg = ws + WS_KB + (size_t)head * (512u << 10) + (size_t)wq * 4096;
    const char* Vg = ws + WS_VB + (size_t)head * (512u << 10) + (size_t)wq * 4096;
    const char* Qg = ws + WS_QB + (size_t)head * (512u << 10) + lane * 16;
    const unsigned l16 = lane * 16, l0 = ldsaddr_of(lds) + wq * 4096;
    const int NA = 32 - 2 * j;
    ldsp kbase = lds + lane * 16, vbase = lds + 32768 + lane * 16;
#define KT(x) ((x) < NA ? (x) : ((x) > 33 ? 0 : (x) - NA))
#define KPIECE(x, i) glds16o<(i) * 1024>(Kg + (size_t)KT(x) * 16384, l16, l0 + ((x) & 1) * 16384)
#define VPIECE(x, i) glds16o<(i) * 1024>(Vg + (size_t)KT(x) * 16384, l16, l0 + 32768 + ((x) & 1) * 16384)
#define KFR(x, h, i) ldsr(kbase + ((x) & 1) * 16384 + (h) * 8192 + (i) * 1024)
#define VFR(x, h, i) ldsr(vbase + ((x) & 1) * 16384 + (h) * 8192 + (i) * 1024)
    bf16x8 qf[8], kf[8], vf[8];
    KPIECE(0, 0); KPIECE(0, 1); KPIECE(0, 2); KPIECE(0, 3);
    VPIECE(0, 0); VPIECE(0, 1); VPIECE(0, 2); VPIECE(0, 3);
    KPIECE(1, 0); KPIECE(1, 1); KPIECE(1, 2); KPIECE(1, 3);
#pragma unroll
    for (int s = 0; s < 8; ++s) qf[s] = *(const bf16x8*)(Qg + (size_t)(((15 - j) * 4 + wq) * 8 + s) * 1024);
    asm volatile("" :: "v"(qf[0]), "v"(qf[1]), "v"(qf[2]), "v"(qf[3]), "v"(qf[4]), "v"(qf[5]), "v"(qf[6]), "v"(qf[7]));
    WAIT_V(0); BARF;
    f32x16 o[4]; o[0] = splat16(0.f); o[1] = o[0]; o[2] = o[0]; o[3] = o[0];
    f32x16 sA, sB, negm;
    float mref, l = 0.f;
    {
#pragma unroll
        for (int i = 0; i < 8; ++i) kf[i] = KFR(0, 0, i);
        sA = splat16(0.f);
#pragma unroll
        for (int i = 0; i < 8; ++i) sA = MFMA32(kf[i], qf[i], sA);
#pragma unroll
        for (int i = 0; i < 8; ++i) kf[i] = KFR(0, 1, i);
        float m = fmaxf(sA[0], sA[1]);
#pragma unroll
        for (int r = 2; r < 16; r += 2) m = fmaxf(fmaxf(m, sA[r]), sA[r + 1]);
        mref = swap32max(m);
        sA -= mref; negm = splat16(-mref); asm volatile("" : "+v"(negm));
    }
    u32x4 pw0, pw1;
#define EXPL(A, e) do { A[e] = __builtin_amdgcn_exp2f(A[e]); l += A[e]; } while (0)
#define SM2(A, e, PW, w) do { EXPL(A, e); EXPL(A, (e) + 1); PW[w] = cvtpk(A[e], A[(e) + 1]); asm volatile("" : "+v"(PW), "+v"(l)); } while (0)
#define PK(A, e, PW, w) do { PW[w] = cvtpk(A[e], A[(e) + 1]); asm volatile("" : "+v"(PW), "+v"(l)); } while (0)
#define PINL asm volatile("" : "+v"(l))
#define FENCE do { asm volatile("" ::: "memory"); __builtin_amdgcn_sched_barrier(0); } while (0)
#define PF(k) __builtin_bit_cast(bf16x8, pw##k)
#define MX(a, b, c, d) do { ma = fmaxf(fmaxf(ma, a), b); mb = fmaxf(fmaxf(mb, c), d); asm volatile("" : "+v"(ma), "+v"(mb)); } while (0)
#define EX(A, e) do { A[e] = __builtin_amdgcn_exp2f(A[e]); asm volatile("" : "+v"(A[e])); } while (0)
#define AD(A, e) do { l += A[e]; } while (0)
#define HSTEP(A, B, u, H, QRL, DEC) do { \
        const int xn = (u) + (H), hn = (H) ^ 1;                \
        if (!(H)) { asm volatile("s_waitcnt vmcnt(0) lgkmcnt(0)\n\ts_barrier" ::: "memory"); FENCE; } \
        vf[0] = VFR(u, H, 0); B = MFMA32(kf[0], qf[0], negm); asm volatile("" : "+v"(B)); if (!(H)) VPIECE((u) + 1, 3); EX(A, 0); EX(A, 1); FENCE; \
        vf[1] = VFR(u, H, 1); B = MFMA32(kf[1], qf[1], B); asm volatile("" : "+v"(B));    AD(A, 0); AD(A, 1); PK(A, 0, pw0, 0); EX(A, 2); FENCE; \
        vf[2] = VFR(u, H, 2); B = MFMA32(kf[2], qf[2], B); asm volatile("" : "+v"(B));    if (!(H)) VPIECE((u) + 1, 0); AD(A, 2); EX(A, 3); PINL; FENCE; \
        vf[3] = VFR(u, H, 3); B = MFMA32(kf[3], qf[3], B); asm volatile("" : "+v"(B));    AD(A, 3); PK(A, 2, pw0, 1); EX(A, 4); FENCE; \
        vf[4] = VFR(u, H, 4); B = MFMA32(kf[4], qf[4], B); asm volatile("" : "+v"(B));    if (!(H)) VPIECE((u) + 1, 1); AD(A, 4); EX(A, 5); PINL; FENCE; \
        vf[5] = VFR(u, H, 5); B = MFMA32(kf[5], qf[5], B); asm volatile("" : "+v"(B));    AD(A, 5); PK(A, 4, pw0, 2); EX(A, 6); FENCE; \
        vf[6] = VFR(u, H, 6); B = MFMA32(kf[6], qf[6], B); asm volatile("" : "+v"(B));    if (!(H)) VPIECE((u) + 1, 2); AD(A, 6); EX(A, 7); PINL; FENCE; \
        vf[7] = VFR(u, H, 7); B = MFMA32(kf[7], qf[7], B); asm volatile("" : "+v"(B));    AD(A, 7); PK(A, 6, pw0, 3); EX(A, 8); EX(A, 9); FENCE; \
        if (QRL) {      \
            const char* qn = ws + WS_QB + (size_t)head * (512u << 10) + (size_t)(j * 4 + wq) * 8192; \
            asm volatile("global_load_dwordx4 %0, %8, %9\n\tglobal_load_dwordx4 %1, %8, %9 offset:1024\n\tglobal_load_dwordx4 %2, %8, %9 offset:2048\n\tglobal_load_dwordx4 %3, %8, %9 offset:3072\n\t" \
                         "global_load_dwordx4 %4, %8, %10\n\tglobal_load_dwordx4 %5, %8, %10 offset:1024\n\tglobal_load_dwordx4 %6, %8, %10 offset:2048\n\tglobal_load_dwordx4 %7, %8, %10 offset:3072" \
                         : "+v"(qf[0]), "+v"(qf[1]), "+v"(qf[2]), "+v"(qf[3]), "+v"(qf[4]), "+v"(qf[5]), "+v"(qf[6]), "+v"(qf[7]) : "v"(l16), "s"(qn), "s"(qn + 4096) : "memory"); \
        } \
        kf[0] = KFR((u) + 1, H, 0); kf[1] = KFR((u) + 1, H, 1); o[0] = MFMA32(vf[0], PF(0), o[0]); asm volatile("" : "+v"(o[0])); AD(A, 8); AD(A, 9); PK(A, 8, pw1, 0); EX(A, 10); EX(A, 11); FENCE; \
        kf[2] = KFR((u) + 1, H, 2); kf[3] = KFR((u) + 1, H, 3); o[1] = MFMA32(vf[1], PF(0), o[1]); asm volatile("" : "+v"(o[1])); AD(A, 10); AD(A, 11); PK(A, 10, pw1, 1); EX(A, 12); EX(A, 13); FENCE; \
        kf[4] = KFR((u) + 1, H, 4); kf[5] = KFR((u) + 1, H, 5); o[2] = MFMA32(vf[2], PF(0), o[2]); asm volatile("" : "+v"(o[2])); AD(A, 12); AD(A, 13); PK(A, 12, pw1, 2); EX(A, 14); EX(A, 15); FENCE; \
        kf[6] = KFR((u) + 1, H, 6); kf[7] = KFR((u) + 1, H, 7); o[3] = MFMA32(vf[3], PF(0), o[3]); asm volatile("" : "+v"(o[3])); AD(A, 14); AD(A, 15); PK(A, 14, pw1, 3); FENCE; \
        { const int dt = (xn < NA ? NA : 34) - xn;             \
          if (dt <= 2) { \
            const int dq = 32 * wq + r32 - 64 * (2 - dt) - 32 * hn - 4 * hh; \
            _Pragma("unroll") for (int r = 0; r < 16; ++r) { const int c = (r & 3) + 8 * (r >> 2); if (c > dq) B[r] = -INFINITY; } \
        } } \
        float ma = fmaxf(fmaxf(B[0], B[1]), B[2]), mb = fmaxf(fmaxf(B[3], B[4]), B[5]); \
        o[0] = MFMA32(vf[4], PF(1), o[0]); asm volatile("" : "+v"(o[0])); if (!(H)) KPIECE((u) + 2, 0); MX(B[6], B[7], B[8], B[9]); FENCE; \
        o[1] = MFMA32(vf[5], PF(1), o[1]); asm volatile("" : "+v"(o[1])); if (!(H)) KPIECE((u) + 2, 1); MX(B[10], B[11], B[12], B[13]); FENCE; \
        o[2] = MFMA32(vf[6], PF(1), o[2]); asm volatile("" : "+v"(o[2])); if (!(H)) KPIECE((u) + 2, 2); ma = fmaxf(fmaxf(ma, B[14]), B[15]); ma = fmaxf(ma, mb); asm volatile("" : "+v"(ma)); FENCE; \
        o[3] = MFMA32(vf[7], PF(1), o[3]); asm volatile("" : "+v"(o[3])); if (!(H)) KPIECE((u) + 2, 3); FENCE; \
        if (QRL) asm volatile("s_waitcnt vmcnt(4)" : "+v"(qf[0]), "+v"(qf[1]), "+v"(qf[2]), "+v"(qf[3]), "+v"(qf[4]), "+v"(qf[5]), "+v"(qf[6]), "+v"(qf[7]) :: "memory"); \
        mlast = ma; \
        if (DEC) { if (__any(ma > THR)) { \
            const float dl = fmaxf(swap32max(ma), 0.f), f = __builtin_amdgcn_exp2f(-dl); \
            mref += dl; l *= f; B -= dl; negm -= dl; \
            _Pragma("unroll") for (int d = 0; d < 4; ++d) o[d] *= f; \
        } } \
    } while (0)
    float mlast;
    int u = 0;
    for (; u < NA - 1; ++u) {
        HSTEP(sA, sB, u, 0, false, true);
        HSTEP(sB, sA, u, 1, false, true);
    }
    HSTEP(sA, sB, u, 0, true, true);
    negm = splat16(0.f); asm volatile("" : "+v"(negm));
    HSTEP(sB, sA, u, 1, false, false);
    attn_b_store(ws, o, l, (15 - j) * 128 + 32 * wq + r32, head, hh);
    o[0] = splat16(0.f); o[1] = o[0]; o[2] = o[0]; o[3] = o[0]; l = 0.f;
    mref = swap32max(mlast);
    sA -= mref; negm = splat16(-mref); asm volatile("" : "+v"(negm));
    for (++u; u < 33; ++u) {
        HSTEP(sA, sB, u, 0, false, true);
        HSTEP(sB, sA, u, 1, false, true);
    }
    HSTEP(sA, sB, 33, 0, false, true);
    {
#pragma unroll
        for (int i = 0; i < 8; ++i) vf[i] = VFR(33, 1, i);
#pragma unroll
        for (int e = 0; e < 8; e += 2) SM2(sB, e, pw0, e >> 1);
#pragma unroll
        for (int e = 8; e < 16; e += 2) SM2(sB, e, pw1, (e - 8) >> 1);
#pragma unroll
        for (int d = 0; d < 4; ++d) o[d] = MFMA32(vf[d], PF(0), o[d]);
#pragma unroll
        for (int d = 0; d < 4; ++d) o[d] = MFMA32(vf[4 + d], PF(1), o[d]);
        attn_b_store(ws, o, l, j * 128 + 32 * wq + r32, head, hh);
        WAIT_V(0);
    }
#undef KT
#undef KPIECE
#undef VPIECE
#undef KFR
#undef VFR
#undef EXPL
#undef SM2
#undef PF
#undef MX
#undef HSTEP
#undef EX
#undef AD
#undef PK
#undef PINL
#undef FENCE
}

DI void attn_a(ldsp lds, char* ws, const char* xA, const char* wqA, int rt, int hp, const char* warmB, const char* warmA) {
    const int tid = threadIdx.x, wid = rfl(tid >> 6), lane = tid & 63, r32 = lane & 31, hh = lane >> 5;
    bf16x8 qall[2][2];
    {
        const char* gA = xA + (size_t)rt * 128 * 2048;
        const char* gW = wqA + (size_t)hp * 128;
        const unsigned vA = (unsigned)(tid >> 3) * 2048u + (unsigned)(((tid & 7) ^ ((tid >> 4) & 7)) << 4);
        const unsigned vW = (unsigned)(wid * 8 + ((lane >> 2) & 7)) * 2048u + (unsigned)((lane >> 5) * 64 + (lane & 3) * 16);
        const unsigned lq = ldsaddr_of(lds) + wid * 1024;
#define QSTAGE(t) do { const int _t = (t) < 16 ? (t) : 15; const unsigned _b = lq + ((t) % 3) * 24576; \
            _Pragma("unroll") for (int _i = 0; _i < 4; ++_i) glds16(gA + _t * 128 + _i * 65536, vA, _b + _i * 4096); \
            glds16(gW + (size_t)_t * 131072, vW, _b + 16384); glds16(gW + (size_t)_t * 131072 + 65536, vW, _b + 16384 + 4096); } while (0)
        const int xrow = wid * 32 + r32, xo = xrow * 128, xs = (xrow >> 1) & 7;
        const int wo = 16384 + hh * 1024 + ((lane >> 2) & 3) * 64 + ((lane >> 4) & 1) * 32 + (lane & 3) * 8;
        f32x16 q0 = splat16(0.f), q1 = q0;
        QSTAGE(0); QSTAGE(1);
        for (int t = 0; t < 16; ++t) {
            asm volatile("s_waitcnt vmcnt(6) lgkmcnt(0)\n\ts_barrier" ::: "memory");
            QSTAGE(t + 2);
            ldsp b = lds + (t % 3) * 24576;
#pragma unroll
            for (int s = 0; s < 4; ++s) {
                const bf16x8 xf = ldsr(b + xo + (((2 * s + hh) ^ xs) << 4));
                q0 = MFMA32(ldstr2(b + wo + s * 2048), xf, q0);
                q1 = MFMA32(ldstr2(b + wo + s * 2048 + 512), xf, q1);
            }
        }
#undef QSTAGE
        q0 *= SCALE_A; q1 *= SCALE_A;
        qall[0][0] = packp(q0, 0); qall[0][1] = packp(q0, 8); qall[1][0] = packp(q1, 0); qall[1][1] = packp(q1, 8);
        asm volatile("s_waitcnt vmcnt(0) lgkmcnt(0)\n\ts_barrier" ::: "memory");
    }
    const char* Kg = ws + WS_KA + (size_t)wid * 2048;
    const char* Vg = ws + WS_VA + (size_t)wid * 2048;
    const unsigned l16 = lane * 16, l0 = ldsaddr_of(lds) + wid * 2048;
    constexpr float SHIFT = 40.f;
#define ASTAGE(c) do { const unsigned _b = l0 + ((c) & 3) * 16384; const size_t _o = (size_t)((c) & 15) * 8192; \
        glds16o<0>(Kg + _o, l16, _b); glds16o<1024>(Kg + _o, l16, _b); glds16o<0>(Vg + _o, l16, _b + 8192); glds16o<1024>(Vg + _o, l16, _b + 8192); } while (0)
#define AKF(i, u) ldsr(lds + l16 + (((i) >> 2) & 3) * 16384 + ((i) & 3) * 2048 + (u) * 1024)
#define AVF(i, u) ldsr(lds + l16 + 8192 + (((i) >> 2) & 3) * 16384 + ((i) & 3) * 2048 + (u) * 1024)
#define FENCE do { asm volatile("" ::: "memory"); __builtin_amdgcn_sched_barrier(0); } while (0)
#define EX2(A, e, PW, w, L) do { A[e] = __builtin_amdgcn_exp2f(A[e]); A[(e) + 1] = __builtin_amdgcn_exp2f(A[(e) + 1]); L += A[e]; L += A[(e) + 1]; PW[w] = cvtpk(A[e], A[(e) + 1]); asm volatile("" : "+v"(PW), "+v"(L)); } while (0)
#define BC(x) __builtin_bit_cast(bf16x8, x)
#define PINV(x) asm volatile("" : "+v"(x))
#define ASTEP(A0, A1, B0, B1, PA0, PA1, PA2, PA3, PB0, PB1, PB2, PB3, i) do { \
        float t0 = 0.f, t1 = 0.f; \
        B0 = MFMA32(kf[0], qall[0][0], ng0); PINV(B0); EX2(A0, 0, PA0, 0, t0);  EX2(A0, 2, PA0, 1, t0);  FENCE; \
        B1 = MFMA32(kf[0], qall[1][0], ng1); PINV(B1); EX2(A0, 4, PA0, 2, t0);  EX2(A0, 6, PA0, 3, t0);  FENCE; \
        B0 = MFMA32(kf[1], qall[0][1], B0);  PINV(B0); EX2(A0, 8, PA1, 0, t0);  EX2(A0, 10, PA1, 1, t0); FENCE; \
        B1 = MFMA32(kf[1], qall[1][1], B1);  PINV(B1); EX2(A0, 12, PA1, 2, t0); EX2(A0, 14, PA1, 3, t0); FENCE; \
        kf[0] = AKF((i) + 2, 0); kf[1] = AKF((i) + 2, 1); \
        o0 = MFMA32(vf[0], BC(PB0), o0);     PINV(o0); EX2(A1, 0, PA2, 0, t1);  EX2(A1, 2, PA2, 1, t1);  FENCE; \
        o1 = MFMA32(vf[0], BC(PB2), o1);     PINV(o1); EX2(A1, 4, PA2, 2, t1);  EX2(A1, 6, PA2, 3, t1);  FENCE; \
        o0 = MFMA32(vf[1], BC(PB1), o0);     PINV(o0); EX2(A1, 8, PA3, 0, t1);  EX2(A1, 10, PA3, 1, t1); FENCE; \
        o1 = MFMA32(vf[1], BC(PB3), o1);     PINV(o1); FENCE; \
        vf[0] = AVF(i, 0); vf[1] = AVF(i, 1);          EX2(A1, 12, PA3, 2, t1); EX2(A1, 14, PA3, 3, t1); FENCE; \
        if (__any(!(fmaxf(t0, t1) < 2.f))) {         \
            const f32x16 z = splat16(0.f); const bf16x8 k0 = AKF(i, 0), k1 = AKF(i, 1); \
            A0 = MFMA32(k0, qall[0][0], z); A1 = MFMA32(k0, qall[1][0], z); A0 = MFMA32(k1, qall[0][1], A0); A1 = MFMA32(k1, qall[1][1], A1); \
            const float mn0 = fmaxf(swap32max(rowmax16(A0)), m0), dl0 = mn0 - m0, f0 = __builtin_amdgcn_exp2f(-dl0); \
            const float mn1 = fmaxf(swap32max(rowmax16(A1)), m1), dl1 = mn1 - m1, f1 = __builtin_amdgcn_exp2f(-dl1); \
            m0 = mn0; o0 *= f0; ls0 *= f0; ng0 -= dl0; B0 -= dl0; A0 += ng0; t0 = 0.f; \
            m1 = mn1; o1 *= f1; ls1 *= f1; ng1 -= dl1; B1 -= dl1; A1 += ng1; t1 = 0.f; \
            EX2(A0, 0, PA0, 0, t0); EX2(A0, 2, PA0, 1, t0); EX2(A0, 4, PA0, 2, t0); EX2(A0, 6, PA0, 3, t0); EX2(A0, 8, PA1, 0, t0); EX2(A0, 10, PA1, 1, t0); EX2(A0, 12, PA1, 2, t0); EX2(A0, 14, PA1, 3, t0); \
            EX2(A1, 0, PA2, 0, t1); EX2(A1, 2, PA2, 1, t1); EX2(A1, 4, PA2, 2, t1); EX2(A1, 6, PA2, 3, t1); EX2(A1, 8, PA3, 0, t1); EX2(A1, 10, PA3, 1, t1); EX2(A1, 12, PA3, 2, t1); EX2(A1, 14, PA3, 3, t1); \
        } \
        ls0 += t0; ls1 += t1; \
    } while (0)
    ASTAGE(0); ASTAGE(1);
    asm volatile("s_waitcnt vmcnt(0) lgkmcnt(0)\n\ts_barrier" ::: "memory");
    f32x16 o0 = splat16(0.f), o1 = o0, ng0, ng1, sA0, sA1, sB0, sB1;
    bf16x8 kf[2], vf[2];
    u32x4 pa0, pa1, pa2, pa3, pb0, pb1, pb2, pb3;
    float m0, m1, ls0 = 0.f, ls1 = 0.f;
    {
        kf[0] = AKF(0, 0); kf[1] = AKF(0, 1);
        sA0 = MFMA32(kf[0], qall[0][0], o0); sA1 = MFMA32(kf[0], qall[1][0], o0); sA0 = MFMA32(kf[1], qall[0][1], sA0); sA1 = MFMA32(kf[1], qall[1][1], sA1);
        kf[0] = AKF(1, 0); kf[1] = AKF(1, 1);
        m0 = swap32max(rowmax16(sA0)); m1 = swap32max(rowmax16(sA1));
        ng0 = splat16(-(m0 + SHIFT)); ng1 = splat16(-(m1 + SHIFT)); asm volatile("" : "+v"(ng0), "+v"(ng1));
        sA0 += ng0; sA1 += ng1;
        pb0 = (u32x4){0, 0, 0, 0}; pb1 = pb0; pb2 = pb0; pb3 = pb0;
        vf[0] = __builtin_bit_cast(bf16x8, pb0); vf[1] = vf[0];
    }
    for (int c = 0; c < 16; ++c) {
        if (c == APRIO_OFF) __builtin_amdgcn_s_setprio(0);
        asm volatile("s_waitcnt vmcnt(0) lgkmcnt(0)\n\ts_barrier" ::: "memory");
        ASTAGE(c + 2);
        if (c < 10) glds16((c < 8 ? warmB + c * 4096 : warmA + (c - 8) * 4096) + wid * 1024, l16, ldsaddr_of(lds) + 73728 + wid * 1024);
        FENCE;
        ASTEP(sA0, sA1, sB0, sB1, pa0, pa1, pa2, pa3, pb0, pb1, pb2, pb3, 4 * c);
        ASTEP(sB0, sB1, sA0, sA1, pb0, pb1, pb2, pb3, pa0, pa1, pa2, pa3, 4 * c + 1);
        ASTEP(sA0, sA1, sB0, sB1, pa0, pa1, pa2, pa3, pb0, pb1, pb2, pb3, 4 * c + 2);
        ASTEP(sB0, sB1, sA0, sA1, pb0, pb1, pb2, pb3, pa0, pa1, pa2, pa3, 4 * c + 3);
    }
    o0 = MFMA32(vf[0], BC(pb0), o0); o1 = MFMA32(vf[0], BC(pb2), o1); o0 = MFMA32(vf[1], BC(pb1), o0); o1 = MFMA32(vf[1], BC(pb3), o1);
    const int row = rt * 128 + wid * 32 + r32;
    char* orow = ws + WS_OA + (size_t)((row >> 7) * 16 + hp) * 16384 + (size_t)(row & 127) * 128;
#pragma unroll
    for (int hd = 0; hd < 2; ++hd) {
        const f32x16& o = hd ? o1 : o0;
        const float inv = 1.f / swap32sum(hd ? ls1 : ls0);
        unsigned w[8];
#pragma unroll
        for (int i = 0; i < 4; ++i) { w[2 * i] = cvtpk(o[4 * i] * inv, o[4 * i + 1] * inv); w[2 * i + 1] = cvtpk(o[4 * i + 2] * inv, o[4 * i + 3] * inv); }
#pragma unroll
        for (int k = 0; k < 4; k += 2) {
            const auto x0 = __builtin_amdgcn_permlane32_swap(w[2 * k], w[2 * k + 2], false, false);
            const auto x1 = __builtin_amdgcn_permlane32_swap(w[2 * k + 1], w[2 * k + 3], false, false);
            *(u32x4*)(orow + (((hd * 4 + k + hh) ^ (row & 7)) << 4)) = (u32x4){x0[0], x1[0], x0[1], x1[1]};
        }
    }
    WAIT_V(0);
#undef ASTAGE
#undef AKF
#undef AVF
#undef FENCE
#undef EX2
#undef BC
#undef PINV
#undef ASTEP
}

__global__ __launch_bounds__(256, 2) void k_attn(char* __restrict__ ws, const char* __restrict__ woA, const char* __restrict__ woB, const char* __restrict__ xA, const char* __restrict__ wqA) {
    extern __shared__ __attribute__((aligned(16))) unsigned char smem[];
    ldsp lds = (ldsp)smem;
    const int b = blockIdx.x & 255, xcd = b & 7, idx = b >> 3;
    if (blockIdx.x < 256) {
        attn_b(lds, ws, xcd * 4 + (idx >> 3), idx & 7);
    } else {
        __builtin_amdgcn_s_setprio(1);
        attn_a(lds, ws, xA, wqA, 2 * xcd + (idx >> 4), idx & 15, woB + (size_t)b * 32768, woA + (size_t)b * 8192);
    }
}

constexpr int SPLIT0 = 42;
DI void store_out(char* outp  , const f32x4 (&acc)[4][2]) {
    const int tid = threadIdx.x, wid = rfl(tid >> 6), lane = tid & 63, wr = wid >> 2, wc = wid & 3, fr = lane & 15, fq = lane >> 4;
    const int c8 = (fq >> 1) | ((fq & 1) << 1);
#pragma unroll
    for (int m = 0; m < 4; ++m) {
        const f32x4 v0 = acc[m][0], v1 = acc[m][1];
        const auto r0 = __builtin_amdgcn_permlane16_swap(cvtpk(v0[0], v0[1]), cvtpk(v1[0], v1[1]), false, false);
        const auto r1 = __builtin_amdgcn_permlane16_swap(cvtpk(v0[2], v0[3]), cvtpk(v1[2], v1[3]), false, false);
        *(u32x4*)(outp + (size_t)(wr * 64 + m * 16 + fr) * 4096 + (size_t)(wc * 64 + c8 * 16)) = (u32x4){r0[0], r1[0], r0[1], r1[1]};
    }
}

template <int ROLE>
DI void oproj(ldsp lds, const char* ctx, const char* woB, const char* oa, const char* woA, char* part, unsigned* flag, char* outp) {
    const int tid = threadIdx.x, wid = rfl(tid >> 6), lane = tid & 63, wr = wid >> 2, wc = wid & 3, fr = lane & 15, fq = lane >> 4;
    constexpr int N1 = ROLE ? 64 - SPLIT0 : SPLIT0, NS = ROLE ? N1 + 16 : N1;
    unsigned voffA[2], voffW[2];
#pragma unroll
    for (int i = 0; i < 2; ++i) {
        voffA[i] = (unsigned)(tid * 16 + i * 8192);
        const int kg = (tid >> 7) + 4 * i, ng = (tid >> 5) & 3, r = (tid >> 2) & 7, c = (tid & 3) ^ (((tid >> 7) & 1) << 1);
        voffW[i] = (unsigned)(kg * 8 + r) * 2048u + (unsigned)(ng * 64 + c * 16);
    }
    const int aoff0 = (wr * 64 + fr) * 128 + ((fq ^ (fr & 7)) << 4), aoff1 = aoff0 ^ 64;
    const int boff0 = 16384 + (fq * 4 + wc) * 512 + ((lane >> 2) & 3) * 64 + (((lane & 3) * 8) ^ ((fq & 1) << 5)), boff1 = boff0 ^ 32;
    const __amdgpu_buffer_rsrc_t rs = __builtin_amdgcn_make_buffer_rsrc(part, 0, 65536, 0x00020000);
#define MPIECE(t, i) do { const int _t = (t) < NS ? (t) : NS - 1; const unsigned _b = ldsaddr_of(lds) + ((t) & 3) * 32768 + wid * 1024; \
        if (!ROLE || _t < N1) { \
            if ((i) < 2) glds16(ctx + (size_t)_t * 16384, voffA[(i) & 1], _b + ((i) & 1) * 8192); \
            else glds16(woB + (size_t)_t * 131072, voffW[(i) & 1], _b + 16384 + ((i) & 1) * 8192); \
        } else { \
            if ((i) < 2) glds16(oa + (size_t)(_t - N1) * 16384, voffA[(i) & 1], _b + ((i) & 1) * 8192); \
            else glds16(woA + (size_t)(_t - N1) * 131072, voffW[(i) & 1], _b + 16384 + ((i) & 1) * 8192); \
        } } while (0)
#define MFENCE do { asm volatile("" ::: "memory"); __builtin_amdgcn_sched_barrier(0); } while (0)
#define MRDA(AF, t, m) do { ldsp _b = lds + ((t) & 3) * 32768; AF[m][0] = ldsr(_b + aoff0 + (m) * 2048); AF[m][1] = ldsr(_b + aoff1 + (m) * 2048); } while (0)
#define MRDB(BF, t, k) do { ldsp _b = lds + ((t) & 3) * 32768; BF[0][k] = ldstr2(_b + boff0 + (k) * 8192); BF[1][k] = ldstr2(_b + boff1 + (k) * 8192); } while (0)
#define MM4(AF, BF, k, m0) do { acc[m0][0] = MFMA16(BF[0][k], AF[m0][k], acc[m0][0]); acc[m0][1] = MFMA16(BF[1][k], AF[m0][k], acc[m0][1]); \
        acc[(m0) + 1][0] = MFMA16(BF[0][k], AF[(m0) + 1][k], acc[(m0) + 1][0]); acc[(m0) + 1][1] = MFMA16(BF[1][k], AF[(m0) + 1][k], acc[(m0) + 1][1]); } while (0)
#define MSTEPX(AX, BX, AY, BY, t, VM, LD) do { \
        asm volatile("s_waitcnt vmcnt(" #VM ") lgkmcnt(0)\n\ts_barrier" ::: "memory"); MFENCE; \
        MRDB(BY, (t) + 1, 0); MRDA(AY, (t) + 1, 0); MM4(AX, BX, 0, 0); if (LD) MPIECE((t) + 4, 0); MFENCE; \
        MRDA(AY, (t) + 1, 1); MRDA(AY, (t) + 1, 2); MM4(AX, BX, 0, 2); if (LD) MPIECE((t) + 4, 1); MFENCE; \
        MRDB(BY, (t) + 1, 1); MRDA(AY, (t) + 1, 3); MM4(AX, BX, 1, 0); if (LD) MPIECE((t) + 4, 2); MFENCE; \
        MM4(AX, BX, 1, 2); if (LD) MPIECE((t) + 4, 3); MFENCE; \
    } while (0)
#define MSTEP(AX, BX, AY, BY, t) MSTEPX(AX, BX, AY, BY, t, 8, true)
    f32x4 acc[4][2];
#pragma unroll
    for (int m = 0; m < 4; ++m)
#pragma unroll
        for (int n = 0; n < 2; ++n) acc[m][n] = (f32x4){0.f, 0.f, 0.f, 0.f};
    bf16x8 A0[4][2], B0[2][2], A1[4][2], B1[2][2];
#pragma unroll
    for (int i = 0; i < 4; ++i) MPIECE(0, i);
#pragma unroll
    for (int i = 0; i < 4; ++i) MPIECE(1, i);
#pragma unroll
    for (int i = 0; i < 4; ++i) MPIECE(2, i);
#pragma unroll
    for (int i = 0; i < 4; ++i) MPIECE(3, i);
    WAIT_V(12); BARF;
    MRDB(B0, 0, 0); MRDB(B0, 0, 1); MRDA(A0, 0, 0); MRDA(A0, 0, 1); MRDA(A0, 0, 2); MRDA(A0, 0, 3);
    if constexpr (ROLE == 1) {
        for (int t = 0; t < N1; t += 2) { MSTEP(A0, B0, A1, B1, t); MSTEP(A1, B1, A0, B0, t + 1); }
        f32x4 hold[4][2];
#pragma unroll
        for (int m = 0; m < 4; ++m)
#pragma unroll
            for (int n = 0; n < 2; ++n) { hold[m][n] = acc[m][n]; acc[m][n] = (f32x4){0.f, 0.f, 0.f, 0.f}; }
#define PUB(m) do { __builtin_amdgcn_raw_buffer_store_b128(__builtin_bit_cast(u32x4, hold[m][0]), rs, ((m) * 2) * 8192 + tid * 16, 0, 16); \
                    __builtin_amdgcn_raw_buffer_store_b128(__builtin_bit_cast(u32x4, hold[m][1]), rs, ((m) * 2 + 1) * 8192 + tid * 16, 0, 16); MFENCE; } while (0)
        PUB(0); MSTEPX(A0, B0, A1, B1, N1, 10, true);     PUB(1); MSTEPX(A1, B1, A0, B0, N1 + 1, 12, true);
        PUB(2); MSTEPX(A0, B0, A1, B1, N1 + 2, 14, true); PUB(3); MSTEPX(A1, B1, A0, B0, N1 + 3, 14, true);
#undef PUB
        MSTEPX(A0, B0, A1, B1, N1 + 4, 12, true); MSTEPX(A1, B1, A0, B0, N1 + 5, 10, true);
        MSTEP(A0, B0, A1, B1, N1 + 6);
        if (tid == 0) __hip_atomic_store(flag, 1u, __ATOMIC_RELAXED, __HIP_MEMORY_SCOPE_AGENT);
        MFENCE;
        MSTEP(A1, B1, A0, B0, N1 + 7);
        for (int t = N1 + 8; t < NS - 4; t += 2) { MSTEP(A0, B0, A1, B1, t); MSTEP(A1, B1, A0, B0, t + 1); }
        MSTEPX(A0, B0, A1, B1, NS - 4, 8, false); MSTEPX(A1, B1, A0, B0, NS - 3, 4, false);
        MSTEPX(A0, B0, A1, B1, NS - 2, 0, false); MSTEPX(A1, B1, A0, B0, NS - 1, 0, false);
        store_out(outp, acc);
    } else {
        for (int t = 0; t < NS - 6; t += 2) { MSTEP(A0, B0, A1, B1, t); MSTEP(A1, B1, A0, B0, t + 1); }
        MSTEP(A0, B0, A1, B1, NS - 6);
        if (wid == 0) { while (rfl((int)__hip_atomic_load(flag, __ATOMIC_RELAXED, __HIP_MEMORY_SCOPE_AGENT)) != 1) __builtin_amdgcn_s_sleep(2); }
        MFENCE;
        MSTEP(A1, B1, A0, B0, NS - 5);
        u32x4 pp[4][2];
#pragma unroll
        for (int m = 0; m < 4; ++m)
#pragma unroll
            for (int n = 0; n < 2; ++n) pp[m][n] = __builtin_amdgcn_raw_buffer_load_b128(rs, (m * 2 + n) * 8192 + tid * 16, 0, 16);
        MFENCE;
        MSTEPX(A0, B0, A1, B1, NS - 4, 16, false); MSTEPX(A1, B1, A0, B0, NS - 3, 12, false);
        MSTEPX(A0, B0, A1, B1, NS - 2, 8, false);  MSTEPX(A1, B1, A0, B0, NS - 1, 8, false);
#pragma unroll
        for (int m = 0; m < 4; ++m)
#pragma unroll
            for (int n = 0; n < 2; ++n) acc[m][n] += __builtin_bit_cast(f32x4, pp[m][n]);
        store_out(outp, acc);
    }
    WAIT_V(0);
#undef MPIECE
#undef MFENCE
#undef MRDA
#undef MRDB
#undef MM4
#undef MSTEP
#undef MSTEPX
}

__global__ __launch_bounds__(512, 2) void k_oproj(const char* __restrict__ woA, const char* __restrict__ woB, char* __restrict__ ws, char* __restrict__ out) {
    extern __shared__ __attribute__((aligned(16))) unsigned char smem[];
    ldsp lds = (ldsp)smem;
    const int b = blockIdx.x, xcd = b & 7, idx = b >> 3, role = xcd >> 2;
    const int rt = 4 * (xcd & 3) + (idx & 3), ct = idx >> 2, tile = rt * 8 + ct;
    const char* ctx = ws + WS_CTX + (size_t)rt * 128 * 8192;
    unsigned* flag = (unsigned*)(ws + WS_FLAG + tile * 128);
    char* part = ws + WS_PART + (size_t)tile * 65536;
    if (role == 1) oproj<1>(lds, ctx + SPLIT0 * 16384, woB + (size_t)SPLIT0 * 131072 + ct * 256, ws + WS_OA + (size_t)rt * 128 * 2048, woA + ct * 256, part, flag, out + (size_t)rt * 128 * 4096 + ct * 256);
    else oproj<0>(lds, ctx, woB + ct * 256, nullptr, nullptr, part, flag, out + (size_t)rt * 128 * 4096 + 2048 + ct * 256);
}

extern "C" void kernel_launch(void* const* d_in, const int* in_sizes, int n_in, void* d_out, int out_size, void* d_ws, size_t ws_size, hipStream_t stream) {
    (void)in_sizes; (void)n_in; (void)out_size; (void)ws_size;
    constexpr int LDS1 = 163840, LDS2 = 73728 + 4096, LDS3 = 131072;
    (void)hipFuncSetAttribute((const void*)k_proj, hipFuncAttributeMaxDynamicSharedMemorySize, LDS1);
    (void)hipFuncSetAttribute((const void*)k_attn, hipFuncAttributeMaxDynamicSharedMemorySize, LDS2);
    (void)hipFuncSetAttribute((const void*)k_oproj, hipFuncAttributeMaxDynamicSharedMemorySize, LDS3);
    char* ws = (char*)d_ws;
    k_proj<<<256, 512, LDS1, stream>>>((const char*)d_in[0], (const char*)d_in[1], (const char*)d_in[2], (const char*)d_in[3], (const char*)d_in[5],
                                       (const char*)d_in[6], (const char*)d_in[7], (const char*)d_in[8], (const char*)d_in[9], ws);
    k_attn<<<512, 256, LDS2, stream>>>(ws, (const char*)d_in[4], (const char*)d_in[10], (const char*)d_in[0], (const char*)d_in[1]);
    k_oproj<<<256, 512, LDS3, stream>>>((const char*)d_in[4], (const char*)d_in[10], ws, (char*)d_out);
}
```
